```python
import torch

def _softmax(x, axis=-1):
    return torch.softmax(x, dim=axis)
def _gelu(x):
    return torch.nn.functional.gelu(x, approximate="tanh")
def _silu(x):
    return torch.nn.functional.silu(x)
def _sigmoid(x):
    return torch.sigmoid(x)
def _layernorm(x, axis=-1, eps=1e-5):
    m = x.mean(dim=axis, keepdim=True)
    v = x.var(dim=axis, keepdim=True, unbiased=False)
    return (x - m) / torch.sqrt(v + eps)
def _scatter_add(tgt, idx, src, axis=0):
    return tgt.clone().index_add_(axis, idx, src)
def _segment_sum(src, seg, n):
    out = torch.zeros((n,) + src.shape[1:], dtype=src.dtype)
    return out.index_add_(0, seg, src)
def _pad(x, pw):
    flat = tuple(v for pair in reversed(pw) for v in pair)
    return torch.nn.functional.pad(x, flat)

kernel_name = 'sequential_preLN__sliding_block__glu_gated_glu__prompt'

def setup_inputs():

    _w = (lambda a: (lambda t: t.to(torch.bfloat16) if t.is_floating_point() else t)(torch.as_tensor(a)))
    def _A_setup():
        B, S, D = 1, 2048, 512
        return {
            "x": _w((lambda *s: torch.randn(*s, dtype=torch.bfloat16) * 0.05)(B, S, D)),
            "wqkv": _w((lambda *s: torch.randn(*s, dtype=torch.bfloat16) * 0.05)(D, 3 * D)),
            "wo": _w((lambda *s: torch.randn(*s, dtype=torch.bfloat16) * 0.05)(D, D)),
        }
    _A_in = _A_setup()

    def _B_setup():
        B, S, D, F = 1, 2048, 512, 1376
        return {
            "x": _w((lambda *s: torch.randn(*s, dtype=torch.bfloat16) * 0.05)(B, S, D)),
            "w_gate": _w((lambda *s: torch.randn(*s, dtype=torch.bfloat16) * 0.05)(D, F)),
            "w_up": _w((lambda *s: torch.randn(*s, dtype=torch.bfloat16) * 0.05)(D, F)),
            "w_down": _w((lambda *s: torch.randn(*s, dtype=torch.bfloat16) * 0.05)(F, D)),
        }
    _B_in = _B_setup()

    return {
        "A_x": _A_in["x"],
        "A_wqkv": _A_in["wqkv"],
        "A_wo": _A_in["wo"],
        "B_x": _B_in["x"],
        "B_w_gate": _B_in["w_gate"],
        "B_w_up": _B_in["w_up"],
        "B_w_down": _B_in["w_down"],
    }

def reference(A_x, A_wqkv, A_wo, B_x, B_w_gate, B_w_up, B_w_down):
    def _A_ref(x, wqkv, wo):
        B, S, D = x.shape
        H = 32; Dh = D // H; W = 256
        qkv = (lambda x, *d: x.permute(*d))((x @ wqkv).reshape(B, S, 3, H, Dh), 2, 0, 3, 1, 4)
        q, k, v = qkv[0], qkv[1], qkv[2]
        scores = torch.einsum("bhqd,bhkd->bhqk", q, k) * (Dh ** -0.5)
        idx = torch.arange(S)
        mask = (idx[:, None] - idx[None, :] >= 0) & (idx[:, None] - idx[None, :] < W)
        scores = torch.where(mask[None, None], scores, -1e9)
        p = _softmax(scores, axis=-1)
        o = torch.einsum("bhqk,bhkd->bhqd", p, v)
        return (lambda x, *d: x.permute(*d))(o, 0, 2, 1, 3).reshape(B, S, D) @ wo

    def _B_ref(x, w_gate, w_up, w_down):
        g = torch.einsum("bsd,df->bsf", x, w_gate)
        u = torch.einsum("bsd,df->bsf", x, w_up)
        h = _sigmoid(g) * u
        return torch.einsum("bsf,fd->bsd", h, w_down)

    x = A_x
    h = x + _A_ref(_layernorm(x, axis=-1), A_wqkv, A_wo)
    h2 = h + _B_ref(_layernorm(h, axis=-1), B_w_gate, B_w_up, B_w_down)
    return h2
```

```cpp
#include <hip/hip_runtime.h>
#include <hip/hip_bf16.h>
typedef __hip_bfloat16 bf16;
static __device__ __forceinline__ float b2f(bf16 v){ return __bfloat162float(v);}
static __device__ __forceinline__ bf16 f2b(float v){ return __float2bfloat16(v);}

__global__ void ln_kernel(const bf16* x, bf16* y){
  int r = blockIdx.x; int t = threadIdx.x;
  __shared__ float red[256];
  float a = b2f(x[r*512+t]), b = b2f(x[r*512+256+t]);
  red[t] = a+b; __syncthreads();
  for(int s=128;s>0;s>>=1){ if(t<s) red[t]+=red[t+s]; __syncthreads(); }
  float m = red[0]/512.f; __syncthreads();
  red[t] = (a-m)*(a-m)+(b-m)*(b-m); __syncthreads();
  for(int s=128;s>0;s>>=1){ if(t<s) red[t]+=red[t+s]; __syncthreads(); }
  float v = red[0]/512.f; float rs = 1.f/sqrtf(v+1e-5f);
  y[r*512+t] = f2b((a-m)*rs); y[r*512+256+t] = f2b((b-m)*rs);
}
__global__ void gemm_kernel(const bf16* A, const bf16* B, bf16* C, const bf16* res, int M, int N, int K){
  int n = blockIdx.x*64 + (threadIdx.x&63); int m = blockIdx.y*4 + (threadIdx.x>>6);
  if(n>=N) return;
  float acc=0.f;
  for(int k=0;k<K;k++) acc += b2f(A[m*K+k])*b2f(B[k*N+n]);
  if(res) acc = b2f(f2b(acc)) + b2f(res[m*N+n]);
  C[m*N+n] = f2b(acc);
}
__global__ void attn_kernel(const bf16* qkv, bf16* o){
  int idx = blockIdx.x*256+threadIdx.x;
  int s = idx>>5, h = idx&31;
  float q[16]; for(int d=0;d<16;d++) q[d]=b2f(qkv[s*1536+h*16+d]);
  int k0 = s-255; if(k0<0) k0=0;
  float mx=-1e30f;
  for(int k=k0;k<=s;k++){ float sc=0; for(int d=0;d<16;d++) sc+=q[d]*b2f(qkv[k*1536+512+h*16+d]); sc*=0.25f; mx=fmaxf(mx,sc);}
  float l=0; float acc[16]; for(int d=0;d<16;d++) acc[d]=0;
  for(int k=k0;k<=s;k++){ float sc=0; for(int d=0;d<16;d++) sc+=q[d]*b2f(qkv[k*1536+512+h*16+d]); sc*=0.25f; float p=__expf(sc-mx); l+=p; for(int d=0;d<16;d++) acc[d]+=p*b2f(qkv[k*1536+1024+h*16+d]); }
  for(int d=0;d<16;d++) o[s*512+h*16+d]=f2b(acc[d]/l);
}
__global__ void glu_kernel(const bf16* g, const bf16* u, bf16* hh, int n){
  int i = blockIdx.x*256+threadIdx.x; if(i>=n) return;
  float gg=b2f(g[i]); float s = 1.f/(1.f+__expf(-gg));
  hh[i]=f2b(b2f(f2b(s))*b2f(u[i]));
}
extern "C" void kernel_launch(void* const* d_in, const int* in_sizes, int n_in,
                              void* d_out, int out_size, void* d_ws, size_t ws_size,
                              hipStream_t stream) {
  const bf16* x=(const bf16*)d_in[0]; const bf16* wqkv=(const bf16*)d_in[1]; const bf16* wo=(const bf16*)d_in[2];
  const bf16* wg=(const bf16*)d_in[4]; const bf16* wu=(const bf16*)d_in[5]; const bf16* wd=(const bf16*)d_in[6];
  bf16* ws=(bf16*)d_ws;
  bf16* ln=ws; bf16* qkv=ln+2048*512; bf16* o=qkv+2048*1536; bf16* h=o+2048*512; bf16* g=h+2048*512; bf16* u=g+2048*1376; bf16* hh=u+2048*1376;
  ln_kernel<<<2048,256,0,stream>>>(x,ln);
  gemm_kernel<<<dim3(1536/64,512),256,0,stream>>>(ln,wqkv,qkv,nullptr,2048,1536,512);
  attn_kernel<<<2048*32/256,256,0,stream>>>(qkv,o);
  gemm_kernel<<<dim3(512/64,512),256,0,stream>>>(o,wo,h,x,2048,512,512);
  ln_kernel<<<2048,256,0,stream>>>(h,ln);
  gemm_kernel<<<dim3(22,512),256,0,stream>>>(ln,wg,g,nullptr,2048,1376,512);
  gemm_kernel<<<dim3(22,512),256,0,stream>>>(ln,wu,u,nullptr,2048,1376,512);
  glu_kernel<<<(2048*1376+255)/256,256,0,stream>>>(g,u,hh,2048*1376);
  gemm_kernel<<<dim3(512/64,512),256,0,stream>>>(hh,wd,(bf16*)d_out,h,2048,512,1376);
}
```

```cpp
#include <hip/hip_runtime.h>
#include <hip/hip_bf16.h>
#include <utility>

typedef __bf16 bf16x8 __attribute__((ext_vector_type(8)));
typedef __bf16 bf16x4 __attribute__((ext_vector_type(4)));
typedef __bf16 bf16x2 __attribute__((ext_vector_type(2)));
typedef float f32x16 __attribute__((ext_vector_type(16)));
typedef float f32x4 __attribute__((ext_vector_type(4)));
typedef float f32x2 __attribute__((ext_vector_type(2)));
typedef unsigned u32x4 __attribute__((ext_vector_type(4)));
typedef unsigned u32x2 __attribute__((ext_vector_type(2)));
typedef unsigned long long u64;
#define LAS __attribute__((address_space(3)))
#define GAS __attribute__((address_space(1)))
#define DI __device__ __forceinline__
#define RLX_AGENT __ATOMIC_RELAXED, __HIP_MEMORY_SCOPE_AGENT
#define MFMA(a, b, c) __builtin_amdgcn_mfma_f32_32x32x16_bf16((a), (b), (c), 0, 0, 0)
#define MFMAP(acc, a, b) do { acc = MFMA(a, b, acc); asm volatile("" : "+v"(acc)); } while (0)
#define OPQ(x) asm volatile("" : "+v"(x))
typedef __amdgpu_buffer_rsrc_t rsrc_t;

constexpr unsigned OFF_EPOCH = 0;
constexpr unsigned OFF_READY = 4096;
constexpr unsigned OFF_KF = 16384;
constexpr unsigned OFF_VF = OFF_KF + 32768;
constexpr unsigned OFF_CNT = OFF_VF + 32768;
constexpr unsigned OFF_CSF = 192u << 10;
constexpr unsigned OFF_CS = 200u << 10;
constexpr unsigned OFF_CS2 = 9u * (1u << 20) + (512u << 10);
constexpr unsigned OFF_STAMP = 512u << 10;
constexpr unsigned OFF_K = 1u << 20;
constexpr unsigned OFF_V = 3u << 20;
constexpr unsigned OFF_O = 5u << 20;
constexpr unsigned OFF_H = 7u << 20;
constexpr unsigned OFF_HS = 9u << 20;
constexpr unsigned OFF_HH = 10u << 20;
constexpr unsigned WS_BYTES = 16u << 20;
constexpr unsigned KB = 1024;
constexpr unsigned L_WK = 0, L_WV = 64 * KB, L_XLN = 96 * KB, L_WQA = 128 * KB, L_WQB = 32 * KB;
constexpr unsigned L_K = 0, L_V = 48 * KB, L_QH = 40 * KB, L_ONES = 88 * KB, L_WO = 96 * KB;
constexpr unsigned L_SCR = 144 * KB;
constexpr unsigned LDS_BYTES = 160 * KB;
constexpr unsigned SPIN_CAP = 1u << 18;

#define BAR() do { asm volatile("s_waitcnt lgkmcnt(0)" ::: "memory"); __builtin_amdgcn_s_barrier(); asm volatile("" ::: "memory"); } while (0)
#define VMWAIT(n) asm volatile("s_waitcnt vmcnt(" #n ")" ::: "memory")

#define STAMP(i) do {} while (0)
#define STAMP_END() do {} while (0)

DI unsigned rfl(unsigned x) { return (unsigned)__builtin_amdgcn_readfirstlane((int)x); }
DI void dma16(rsrc_t rs, unsigned voff, unsigned soff, unsigned lds) {
  asm volatile("s_mov_b32 m0, %0\n\ts_nop 0\n\tbuffer_load_dwordx4 %1, %2, %3 offen lds" ::"s"(lds), "v"(voff), "s"(rs), "s"(soff) : "m0", "memory");
}
DI void dma16_sc1(rsrc_t rs, unsigned voff, unsigned soff, unsigned lds) {
  asm volatile("s_mov_b32 m0, %0\n\ts_nop 0\n\tbuffer_load_dwordx4 %1, %2, %3 offen sc1 lds" ::"s"(lds), "v"(voff), "s"(rs), "s"(soff) : "m0", "memory");
}
DI void dma_run4(rsrc_t rs, unsigned voff, unsigned soff, unsigned step, unsigned lds, int n4) {
#pragma unroll 1
  for (int i = 0; i < n4; ++i) {
    asm volatile(
        "s_mov_b32 m0, %[l]\n\ts_nop 0\n\tbuffer_load_dwordx4 %[v], %[r], %[s] offen lds\n\t"
        "s_add_u32 m0, m0, 0x400\n\ts_add_u32 %[s], %[s], %[st]\n\tbuffer_load_dwordx4 %[v], %[r], %[s] offen lds\n\t"
        "s_add_u32 m0, m0, 0x400\n\ts_add_u32 %[s], %[s], %[st]\n\tbuffer_load_dwordx4 %[v], %[r], %[s] offen lds\n\t"
        "s_add_u32 m0, m0, 0x400\n\ts_add_u32 %[s], %[s], %[st]\n\tbuffer_load_dwordx4 %[v], %[r], %[s] offen lds\n\t"
        "s_add_u32 %[s], %[s], %[st]\n\ts_add_u32 %[l], %[l], 0x1000"
        : [s] "+s"(soff), [l] "+s"(lds) : [v] "v"(voff), [r] "s"(rs), [st] "s"(step) : "m0", "scc", "memory");
  }
}
DI void dma_run4_sc1(rsrc_t rs, unsigned voff, unsigned soff, unsigned lds) {
  asm volatile(
      "s_mov_b32 m0, %[l]\n\ts_nop 0\n\tbuffer_load_dwordx4 %[v], %[r], %[s] offen sc1 lds\n\t"
      "buffer_load_dwordx4 %[v], %[r], %[s] offen offset:1024 sc1 lds\n\t"
      "buffer_load_dwordx4 %[v], %[r], %[s] offen offset:2048 sc1 lds\n\t"
      "buffer_load_dwordx4 %[v], %[r], %[s] offen offset:3072 sc1 lds"
      ::[s] "s"(soff), [l] "s"(lds), [v] "v"(voff), [r] "s"(rs) : "m0", "memory");
}
#define RBAR() do { asm volatile("" ::: "memory"); __builtin_amdgcn_s_barrier(); asm volatile("" ::: "memory"); } while (0)
#define SCHED() __builtin_amdgcn_sched_barrier(0)
template <int... I, class F> DI void sfor_(std::integer_sequence<int, I...>, F&& f) { (f(std::integral_constant<int, I>{}), ...); }
template <int N, class F> DI void sfor(F&& f) { sfor_(std::make_integer_sequence<int, N>{}, f); }
template <int N, int D, class LD, class MM, class PRE>
DI void pipe(LD&& ld, MM&& mm, PRE&& pre) {
  bf16x8 buf[D];
  sfor<D>([&](auto I) { pre(I); buf[I.value] = ld(I); });
  SCHED();
  sfor<N>([&](auto S) {
    constexpr int s = S.value;
    mm(S, buf[s % D]);
    if constexpr (s + D < N) { pre(std::integral_constant<int, s + D>{}); buf[s % D] = ld(std::integral_constant<int, s + D>{}); }
    SCHED();
  });
}
DI bf16x8 trfrag(unsigned a0, unsigned a1) {
  bf16x4 lo = __builtin_amdgcn_ds_read_tr16_b64_v4bf16((LAS bf16x4*)a0);
  bf16x4 hi = __builtin_amdgcn_ds_read_tr16_b64_v4bf16((LAS bf16x4*)a1);
  return __builtin_shufflevector(lo, hi, 0, 1, 2, 3, 4, 5, 6, 7);
}
DI bf16x8 ldsfrag(unsigned a) { return *(LAS bf16x8*)a; }
DI void ldsst(unsigned a, u32x4 v) { *(LAS u32x4*)a = v; }
DI unsigned pk2(float a, float b) { f32x2 v = {a, b}; return __builtin_bit_cast(unsigned, __builtin_convertvector(v, bf16x2)); }
DI u32x4 pack8(const f32x16& x, int o) { u32x4 r; r[0] = pk2(x[o], x[o + 1]); r[1] = pk2(x[o + 2], x[o + 3]); r[2] = pk2(x[o + 4], x[o + 5]); r[3] = pk2(x[o + 6], x[o + 7]); return r; }
DI float blo(unsigned u) { return __uint_as_float(u << 16); }
DI float bhi(unsigned u) { return __uint_as_float(u & 0xffff0000u); }
DI bf16x8 asfrag(u32x4 v) { return __builtin_bit_cast(bf16x8, v); }
DI float swap32_add(float a) {
  auto r = __builtin_amdgcn_permlane32_swap(__float_as_uint(a), __float_as_uint(a), false, false);
  return __uint_as_float(r[0]) + __uint_as_float(r[1]);
}
DI float swap32_max(float a) {
  auto r = __builtin_amdgcn_permlane32_swap(__float_as_uint(a), __float_as_uint(a), false, false);
  return fmaxf(__uint_as_float(r[0]), __uint_as_float(r[1]));
}
template <int CTRL> DI float dpp_add(float a) { return a + __uint_as_float(__builtin_amdgcn_update_dpp(0, __float_as_uint(a), CTRL, 0xf, 0xf, true)); }
DI float row16_sum(float a) { a = dpp_add<0x128>(a); a = dpp_add<0x124>(a); a = dpp_add<0x122>(a); a = dpp_add<0x121>(a); return a; }
DI void red8(const float (&v)[8], float& A, float& B) {
  float t[4];
#pragma unroll
  for (int i = 0; i < 4; ++i) {
    auto r = __builtin_amdgcn_permlane32_swap(__float_as_uint(v[2 * i]), __float_as_uint(v[2 * i + 1]), false, false);
    t[i] = __uint_as_float(r[0]) + __uint_as_float(r[1]);
  }
  auto r0 = __builtin_amdgcn_permlane16_swap(__float_as_uint(t[0]), __float_as_uint(t[1]), false, false);
  auto r1 = __builtin_amdgcn_permlane16_swap(__float_as_uint(t[2]), __float_as_uint(t[3]), false, false);
  A = row16_sum(__uint_as_float(r0[0]) + __uint_as_float(r0[1]));
  B = row16_sum(__uint_as_float(r1[0]) + __uint_as_float(r1[1]));
}
DI float rdl(float a, int l) { return __uint_as_float((unsigned)__builtin_amdgcn_readlane((int)__float_as_uint(a), l)); }
DI void bc8(float A, float B, float (&o)[8]) {
  o[0] = rdl(A, 0); o[2] = rdl(A, 16); o[1] = rdl(A, 32); o[3] = rdl(A, 48);
  o[4] = rdl(B, 0); o[6] = rdl(B, 16); o[5] = rdl(B, 32); o[7] = rdl(B, 48);
}
DI void wait_cnt(GAS unsigned* c, unsigned have, unsigned target) {
  unsigned spins = 0;
#pragma unroll 1
  while (have < target) {
    __builtin_amdgcn_s_sleep(1);
    have = rfl(__hip_atomic_load(c, RLX_AGENT));
    if (++spins > SPIN_CAP) break;
  }
  __builtin_amdgcn_fence(__ATOMIC_ACQUIRE, "wavefront");
}
DI unsigned arrive(GAS unsigned* c, int lane) {
  unsigned r = 0;
  if (lane == 0) r = __hip_atomic_fetch_add(c, 1u, RLX_AGENT) + 1u;
  r = rfl(r);
  asm volatile("" ::"s"(r));
  return r;
}

__host__ __device__ constexpr int Dm(int m) { return m < 32 ? ((43 * (m & 15)) >> 4) + (m >> 4) : (4 + 8 * ((m - 32) >> 1) + 3 * ((m - 32) & 1) < 42 ? 4 + 8 * ((m - 32) >> 1) + 3 * ((m - 32) & 1) : 42); }

template <bool FULL, class MID, class HOOK, class ST>
DI u32x4 attention(const unsigned ka, const unsigned va, const bf16x8 q, const int n0, const int e, const int r, const int h, const bool helper, MID&& mid, HOOK&& hook, ST&& stamp) {
  bf16x8 kf[9];
#pragma unroll
  for (int n = 0; n < 9; ++n) kf[n] = ldsfrag(ka + n * 4096);
  f32x16 zero;
#pragma unroll
  for (int i = 0; i < 16; ++i) zero[i] = 0.f;
  f32x16 sm = MFMA(kf[8], q, zero);
  f32x16 sa, sb;
  if (FULL) {
    sb = MFMA(kf[0], q, zero);
    sa = MFMA(kf[1], q, zero);
    OPQ(sa);
  } else {
#pragma unroll
    for (int i = 0; i < 16; ++i) sb[i] = -1e30f;
  }
#pragma unroll
  for (int i = 0; i < 16; ++i) { const int kl = (i & 3) + 8 * (i >> 2) + 4 * h; sm[i] = (kl <= r) ? sm[i] : sb[i]; }
  float mx = sm[0], mx2 = sm[1];
#pragma unroll
  for (int i = 2; i < 16; i += 2) mx = fmaxf(fmaxf(mx, sm[i]), sm[i + 1]);
  if (FULL) {
    SCHED();
#pragma unroll
    for (int n = 1; n < 8; ++n) {
      if (n & 1) {
        if (n < 7) { sb = MFMA(kf[n + 1], q, zero); OPQ(sb); }
        SCHED();
#pragma unroll
        for (int i = 0; i < 16; i += 4) { mx = fmaxf(fmaxf(mx, sa[i]), sa[i + 1]); mx2 = fmaxf(fmaxf(mx2, sa[i + 2]), sa[i + 3]); }
      } else {
        sa = MFMA(kf[n + 1], q, zero); OPQ(sa);
        SCHED();
#pragma unroll
        for (int i = 0; i < 16; i += 4) { mx = fmaxf(fmaxf(mx, sb[i]), sb[i + 1]); mx2 = fmaxf(fmaxf(mx2, sb[i + 2]), sb[i + 3]); }
      }
      SCHED();
      if (n == 3) mid();
    }
  } else {
    mid();
#pragma unroll
    for (int n = 1; n < 8; ++n) {
      if (n >= n0) {
        const f32x16 s = MFMA(kf[n], q, zero);
#pragma unroll
        for (int i = 0; i < 16; ++i) mx = fmaxf(mx, s[i]);
      }
    }
  }
  mx = swap32_max(fmaxf(mx, mx2));
  f32x16 negm;
#pragma unroll
  for (int i = 0; i < 16; ++i) negm[i] = -mx;
  if (FULL) { sa = MFMA(kf[1], q, negm); OPQ(sa); }
  u32x4 p0, p1, d0, d1;
  {
    f32x16 p;
#pragma unroll
    for (int i = 0; i < 16; ++i) p[i] = __builtin_amdgcn_exp2f(sm[i] - mx);
    p0 = pack8(p, 0); p1 = pack8(p, 8);
#pragma unroll
    for (int k = 0; k < 4; ++k) {
      const int i0 = 2 * k, i1 = 8 + 2 * k;
      const int ka0 = (i0 & 3) + 8 * (i0 >> 2) + 4 * h, ka1 = (i1 & 3) + 8 * (i1 >> 2) + 4 * h;
      const unsigned m0 = (ka0 <= r ? 0xFFFFu : 0u) | (ka0 + 1 <= r ? 0xFFFF0000u : 0u);
      const unsigned m1 = (ka1 <= r ? 0xFFFFu : 0u) | (ka1 + 1 <= r ? 0xFFFF0000u : 0u);
      d0[k] = p0[k] & m0; d1[k] = p1[k] & m1;
    }
    p0 ^= d0; p1 ^= d1;
  }
  OPQ(p0); OPQ(p1); OPQ(d0); OPQ(d1);
  stamp(16);
  if (helper) VMWAIT(0);
  RBAR();
  stamp(7);
  const bool ones = ((r >> 4) != e);
  unsigned vp = ones ? L_ONES : va;
  const unsigned i1k = ones ? 0u : 1024u, i3k = ones ? 0u : 3072u;
  f32x16 o = zero;
  if (FULL) {
    const unsigned v8 = vp + 32 * i1k;
    const bf16x8 w80 = ldsfrag(v8), w81 = ldsfrag(v8 + i1k);
    const bf16x8 w00 = ldsfrag(vp); vp += i1k;
    const bf16x8 w01 = ldsfrag(vp); vp += i3k;
    bf16x8 v0 = ldsfrag(vp); vp += i1k;
    bf16x8 v1 = ldsfrag(vp); vp += i3k;
    o = MFMA(w80, asfrag(d0), o);
    o = MFMA(w81, asfrag(d1), o);
    o = MFMA(w00, asfrag(p0), o);
    o = MFMA(w01, asfrag(p1), o);
    OPQ(o);
    SCHED();
#pragma unroll
    for (int n = 1; n < 8; ++n) {
      bf16x8 n0f, n1f;
      if (n < 7) {
        if (n & 1) { sb = MFMA(kf[n + 1], q, negm); OPQ(sb); } else { sa = MFMA(kf[n + 1], q, negm); OPQ(sa); }
        n0f = ldsfrag(vp); vp += i1k;
        n1f = ldsfrag(vp); vp += i3k;
        hook(n - 1);
      }
      SCHED();
      f32x16& s = (n & 1) ? sa : sb;
#pragma unroll
      for (int i = 0; i < 8; ++i) s[i] = __builtin_amdgcn_exp2f(s[i]);
      MFMAP(o, v0, asfrag(pack8(s, 0)));
      SCHED();
#pragma unroll
      for (int i = 8; i < 16; ++i) s[i] = __builtin_amdgcn_exp2f(s[i]);
      MFMAP(o, v1, asfrag(pack8(s, 8)));
      SCHED();
      v0 = n0f; v1 = n1f;
    }
  } else {
#pragma unroll
    for (int n = 0; n < 6; ++n) hook(n);
    o = MFMA(ldsfrag(vp + 32 * i1k), asfrag(d0), o);
    o = MFMA(ldsfrag(vp + 33 * i1k), asfrag(d1), o);
#pragma unroll
    for (int n = 1; n < 8; ++n) {
      if (n >= n0) {
        f32x16 p = MFMA(kf[n], q, negm);
#pragma unroll
        for (int i = 0; i < 16; ++i) p[i] = __builtin_amdgcn_exp2f(p[i]);
        o = MFMA(ldsfrag(vp + (4 * n) * i1k), asfrag(pack8(p, 0)), o);
        o = MFMA(ldsfrag(vp + (4 * n + 1) * i1k), asfrag(pack8(p, 8)), o);
      }
    }
  }
  u32x4 of;
  if (e == 0) {
    const float il = __builtin_amdgcn_rcpf(o[8]);
#pragma unroll
    for (int i = 0; i < 8; ++i) o[i] *= il;
    of = pack8(o, 0);
    OPQ(of);
  } else {
    const float il = __builtin_amdgcn_rcpf(o[0]);
#pragma unroll
    for (int i = 8; i < 16; ++i) o[i] *= il;
    of = pack8(o, 8);
    OPQ(of);
  }
  return of;
}

DI void ln8(const u32x4* xr, const unsigned wb, const int lane) {
  float s1[8], s2[8], rs[8], nm[8];
  const bf16x2 one2 = __builtin_bit_cast(bf16x2, 0x3F803F80u);
#pragma unroll
  for (int i = 0; i < 8; ++i) {
    float a = 0.f, q = 0.f;
#pragma unroll
    for (int k = 0; k < 4; ++k) {
      const unsigned xu = xr[i][k];
      const bf16x2 v = __builtin_bit_cast(bf16x2, xu);
      a = __builtin_amdgcn_fdot2_f32_bf16(v, one2, a, false);
      q = __builtin_amdgcn_fdot2_f32_bf16(v, v, q, false);
    }
    s1[i] = a; s2[i] = q;
  }
  float A1, B1, A2, B2;
  red8(s1, A1, B1);
  red8(s2, A2, B2);
  A1 *= (1.f / 512.f); B1 *= (1.f / 512.f);
  A2 = __builtin_amdgcn_rsqf(fmaxf(fmaf(-A1, A1, A2 * (1.f / 512.f)), 0.f) + 1e-5f);
  B2 = __builtin_amdgcn_rsqf(fmaxf(fmaf(-B1, B1, B2 * (1.f / 512.f)), 0.f) + 1e-5f);
  bc8(A2, B2, rs);
  bc8(-A1 * A2, -B1 * B2, nm);
#pragma unroll
  for (int i = 0; i < 8; ++i) {
    u32x4 o;
#pragma unroll
    for (int k = 0; k < 4; ++k) o[k] = pk2(fmaf(blo(xr[i][k]), rs[i], nm[i]), fmaf(bhi(xr[i][k]), rs[i], nm[i]));
    ldsst(wb + (unsigned)((i ^ (lane & 7)) << 4), o);
  }
}
DI void poll_tag(const GAS u64* p, const u64 T) {
  unsigned spins = 0;
#pragma unroll 1
  for (;;) {
    const u64 v = __hip_atomic_load(p, RLX_AGENT);
    if (__all(v == T)) break;
    __builtin_amdgcn_s_sleep(1);
    if (++spins > SPIN_CAP) break;
  }
  asm volatile("" ::: "memory");
}
DI void signal(GAS unsigned* c, int lane) { if (lane == 0) (void)__hip_atomic_fetch_add(c, 1u, RLX_AGENT); }

__global__ void __launch_bounds__(512, 2)
fused_block(const void* __restrict__ gx, const void* __restrict__ gwqkv, const void* __restrict__ gwo, const void* __restrict__ gwg,
            const void* __restrict__ gwu, const void* __restrict__ gwd, void* __restrict__ gout, char* ws) {
  extern __shared__ __attribute__((aligned(16))) char smem[];
  const int lane = threadIdx.x & 63;
  const int wave = (int)rfl(threadIdx.x >> 6);
  STAMP(0);
  const int b = blockIdx.x, xcd = b & 7, idx = b >> 3;
  const int G = xcd * 2 + (idx >> 4), j = idx & 15, t = j >> 3, c = j & 7, H = 2 * G + t;
  const int r = lane & 31, h = lane >> 5;
  constexpr unsigned LB = 0;
  asm volatile("" ::"v"(smem));
  const rsrc_t rX = __builtin_amdgcn_make_buffer_rsrc((void*)gx, 0, 2048 * 512 * 2, 0x00020000);
  const rsrc_t rWQKV = __builtin_amdgcn_make_buffer_rsrc((void*)gwqkv, 0, 512 * 1536 * 2, 0x00020000);
  const rsrc_t rWO = __builtin_amdgcn_make_buffer_rsrc((void*)gwo, 0, 512 * 512 * 2, 0x00020000);
  const rsrc_t rWG = __builtin_amdgcn_make_buffer_rsrc((void*)gwg, 0, 512 * 1376 * 2, 0x00020000);
  const rsrc_t rWU = __builtin_amdgcn_make_buffer_rsrc((void*)gwu, 0, 512 * 1376 * 2, 0x00020000);
  const rsrc_t rWD = __builtin_amdgcn_make_buffer_rsrc((void*)gwd, 0, 1376 * 512 * 2, 0x00020000);
  const rsrc_t rOUT = __builtin_amdgcn_make_buffer_rsrc(gout, 0, 2048 * 512 * 2, 0x00020000);
  const rsrc_t rWS = __builtin_amdgcn_make_buffer_rsrc((void*)ws, 0, WS_BYTES, 0x00020000);

  const unsigned chsw = (unsigned)(((lane & 7) ^ (((lane >> 4) & 1) << 2)) * 16);
  const unsigned v1k = (unsigned)(lane >> 3) * 1024u + chsw;
  const int trq = (lane & 15) >> 2, trp = lane & 3, trcb = (lane >> 4) & 1;
  const int tau = (wave & 3) >> 1, hp = wave & 1;
  const int tt = 2 * H + tau;
  GAS unsigned* cntT = (GAS unsigned*)(ws + OFF_CNT + (unsigned)tt * 1024u);
  u32x4 qf;
  u64 T;

  const unsigned vq = (unsigned)(lane >> 3) * 3072u + chsw;
  {
    asm volatile("s_load_dwordx2 %0, %1, 0x0" : "=s"(T) : "s"(ws));
    u32x4 xr[8];
    const unsigned vo = lane * 16, so = (unsigned)(64 * H + 8 * wave) * 1024u;
#pragma unroll
    for (int i = 0; i < 8; ++i)
      asm volatile("buffer_load_dwordx4 %0, %1, %2, %3 offen offset:%4" : "=v"(xr[i]) : "v"(vo + (i >> 2) * 4096), "s"(rX), "s"(so), "i"((i & 3) * 1024) : "memory");
#define PFN 0
    RBAR();
    dma_run4(rWQKV, vq, (unsigned)(64 * wave) * 3072u + (512u + 64u * c) * 2u, 8 * 3072, LB + L_WK + (unsigned)(64 * wave) * 128u, 2);
    asm volatile("s_waitcnt vmcnt(%9) lgkmcnt(0)" : "+v"(xr[0]), "+v"(xr[1]), "+v"(xr[2]), "+v"(xr[3]), "+v"(xr[4]), "+v"(xr[5]), "+v"(xr[6]), "+v"(xr[7]), "+s"(T) : "i"(8 + PFN) : "memory");
    T += 1ull;
    STAMP(1);
    ln8(xr, LB + L_XLN + (unsigned)(wave >> 2) * 32768u + (unsigned)(lane >> 1) * 1024u + (unsigned)(lane & 1) * 512u + (unsigned)(wave & 3) * 128u, lane);
    STAMP(30);
  }
  const int Hp = H - 8 + wave;

  if (wave < 4) {
    dma_run4(rWQKV, vq, (unsigned)(64 * wave) * 3072u + (1024u + 64u * c) * 2u, 8 * 3072, LB + L_WV + (unsigned)(64 * wave) * 128u, 2);
    VMWAIT(8);
    BAR();
    STAMP(2);
    const unsigned trn = LB + (unsigned)((8 * h + trq) * 128 + ((hp ^ (trq >> 1)) * 64) + trcb * 32 + trp * 8);
    unsigned trn2 = trn + 65536u, trn3 = trn + L_WQA;
    OPQ(trn2); OPQ(trn3);
    bf16x8 xf[32];
    const unsigned xb = LB + L_XLN + (unsigned)tau * 32768u + (unsigned)h * 512u;
    auto ldx = [&](int s) { xf[s] = ldsfrag(xb + (unsigned)s * 1024u + (unsigned)((r ^ ((2 * (s & 3) + h) & 7)) << 4)); };
    f32x16 aK, aV, aQ;
#pragma unroll
    for (int i = 0; i < 16; ++i) { aK[i] = 0.f; aV[i] = 0.f; aQ[i] = 0.f; }
    u32x4 kf0, kf1, vf0, vf1;
#pragma unroll
    for (int s = 0; s < 4; ++s) ldx(s);
    const unsigned kvo = (unsigned)((tt * 8 + c) * 4 + hp * 2) * 1024u + lane * 16;
    pipe<32, 4>([&](auto I_) __attribute__((always_inline)) { constexpr int i = I_.value; return trfrag(trn + i * 2048, trn + i * 2048 + 512); },
                [&](auto I_, const bf16x8& w) __attribute__((always_inline)) {
                  constexpr int i = I_.value;
                  MFMAP(aK, w, xf[i]);
                  if constexpr (i + 4 < 32) ldx(i + 4);
                },
                [&](auto) {});
    kf0 = pack8(aK, 0); kf1 = pack8(aK, 8);
    __builtin_amdgcn_raw_buffer_store_b128(kf0, rWS, OFF_K + kvo, 0, 16);
    __builtin_amdgcn_raw_buffer_store_b128(kf1, rWS, OFF_K + kvo + 1024, 0, 16);
    STAMP(3);
    VMWAIT(2);
    RBAR();
    STAMP(27);
    pipe<64, 5>(
        [&](auto I_) __attribute__((always_inline)) {
          constexpr int i = I_.value;
          const unsigned a = i < 32 ? trn2 + i * 2048 : i < 48 ? trn3 + (i - 32) * 2048 : trn + L_WQB + (i - 48) * 2048;
          return trfrag(a, a + 512);
        },
        [&](auto I_, const bf16x8& w) __attribute__((always_inline)) {
          constexpr int i = I_.value;
          if constexpr (i < 32) MFMAP(aV, xf[i], w);
          else MFMAP(aQ, w, xf[i - 32]);
          if (i == 26) {
            VMWAIT(0);
            if (lane == 0) __hip_atomic_store((GAS u64*)(ws + OFF_KF + (unsigned)(H * 8 + c) * 128u) + wave, T, RLX_AGENT);
          }
          if (i == 33) {
            vf0 = pack8(aV, 0); vf1 = pack8(aV, 8);
            __builtin_amdgcn_raw_buffer_store_b128(vf0, rWS, OFF_V + kvo, 0, 16);
            __builtin_amdgcn_raw_buffer_store_b128(vf1, rWS, OFF_V + kvo + 1024, 0, 16);
            STAMP(4);
          }
          if (i == 60) {
            VMWAIT(0);
            if (lane == 0) __hip_atomic_store((GAS u64*)(ws + OFF_VF + (unsigned)(H * 8 + c) * 128u) + wave, T, RLX_AGENT);
          }
        },
        [&](auto I_) __attribute__((always_inline)) {
          constexpr int i = I_.value;
          if (i == 16) RBAR();
          if (i == 32) RBAR();
        });
    STAMP(5);
    RBAR();
#pragma unroll
    for (int i = 0; i < 16; ++i) aQ[i] *= 0.25f * 1.44269504088896f;
    qf = pack8(aQ, 0);
    const u32x4 qf1 = pack8(aQ, 8);
    ldsst(LB + L_K + (unsigned)((8 + tau) * 4 + hp * 2) * 1024u + lane * 16, kf0);
    ldsst(LB + L_K + (unsigned)((8 + tau) * 4 + hp * 2 + 1) * 1024u + lane * 16, kf1);
    ldsst(LB + L_V + (unsigned)((8 + tau) * 4 + hp * 2) * 1024u + lane * 16, vf0);
    ldsst(LB + L_V + (unsigned)((8 + tau) * 4 + hp * 2 + 1) * 1024u + lane * 16, vf1);
    ldsst(LB + L_QH + (unsigned)wave * 1024u + lane * 16, qf1);
    { const u32x4 one4 = {0x3F803F80u, 0x3F803F80u, 0x3F803F80u, 0x3F803F80u}; ldsst(LB + L_ONES, one4); }
    dma_run4(rWO, v1k, (unsigned)(128 * wave) * 1024u + 128u * c, 8 * 1024, LB + L_WO + (unsigned)(128 * wave) * 128u, 4);
    BAR();
  } else {
    const int hw = wave - 4;
    VMWAIT(0);
    BAR();
    u32x4 st[24];
#pragma unroll
    for (int i = 0; i < 24; ++i) {
      const unsigned so = (unsigned)((i < 8 || i >= 16 ? 256 : 0) + 64 * hw + 8 * (i & 7)) * 3072u + ((i < 8 ? 1024u : 0u) + 64u * c) * 2u;
      asm volatile("buffer_load_dwordx4 %0, %1, %2, %3 offen" : "=v"(st[i]) : "v"(vq), "s"(rWQKV), "s"(so) : "memory");
    }
    STAMP(3);
    RBAR();
    asm volatile("s_waitcnt vmcnt(16)" : "+v"(st[0]), "+v"(st[1]), "+v"(st[2]), "+v"(st[3]), "+v"(st[4]), "+v"(st[5]), "+v"(st[6]), "+v"(st[7])::"memory");
#pragma unroll
    for (int i = 0; i < 8; ++i) ldsst(LB + L_WV + (unsigned)(256 + 64 * hw + 8 * i) * 128u + lane * 16, st[i]);
    BAR();
    asm volatile("s_waitcnt vmcnt(0)" : "+v"(st[8]), "+v"(st[9]), "+v"(st[10]), "+v"(st[11]), "+v"(st[12]), "+v"(st[13]), "+v"(st[14]), "+v"(st[15]),
                 "+v"(st[16]), "+v"(st[17]), "+v"(st[18]), "+v"(st[19]), "+v"(st[20]), "+v"(st[21]), "+v"(st[22]), "+v"(st[23])::"memory");
#pragma unroll
    for (int i = 0; i < 8; ++i) {
      ldsst(LB + L_WQA + (unsigned)(64 * hw + 8 * i) * 128u + lane * 16, st[8 + i]);
      ldsst(LB + L_WQB + (unsigned)(64 * hw + 8 * i) * 128u + lane * 16, st[16 + i]);
    }
    BAR();
    if (j == 0 && hw == 0) {
      if (lane < 32) __hip_atomic_store((GAS unsigned*)(ws + OFF_CNT + (unsigned)(32 * G + lane) * 128u), 0u, RLX_AGENT);
      VMWAIT(0);
      if (lane == 0) __hip_atomic_store((GAS u64*)(ws + OFF_READY + (unsigned)G * 256u), T, RLX_AGENT);
    }
    if (Hp >= 0) {
      poll_tag((const GAS u64*)(ws + OFF_KF + (unsigned)(Hp * 8 + c) * 128u) + (lane & 3), T);
      dma_run4_sc1(rWS, lane * 16, OFF_K + (unsigned)((2 * Hp) * 8 + c) * 4096u, LB + L_K + (unsigned)(8 * hw) * 1024u);
      dma_run4_sc1(rWS, lane * 16, OFF_K + (unsigned)((2 * Hp + 1) * 8 + c) * 4096u, LB + L_K + (unsigned)(8 * hw + 4) * 1024u);
    }
    STAMP(5);
    RBAR();
    VMWAIT(0);
    RBAR();
    qf = *(LAS u32x4*)(LB + L_QH + (unsigned)hw * 1024u + lane * 16);
  }
  STAMP(6);

  const int du0 = (43 * j) >> 4, ndu = ((43 * (j + 1)) >> 4) - du0;
  unsigned v3g[3], v3u[3];
#pragma unroll
  for (int i = 0; i < 3; ++i) {
    const unsigned qd = (unsigned)(i * 64 + lane), row = qd / 12u, ch = qd - row * 12u;
    v3g[i] = row * 2752u + ch * 16u;
    v3u[i] = row * 2752u + (ch ^ 2u) * 16u;
  }
  {
    const int e = wave >> 2;
    const unsigned ka = LB + L_K + (unsigned)(tau * 4 + hp * 2 + e) * 1024u + lane * 16;
    const unsigned va = LB + L_V + (unsigned)(tau * 4 + hp * 2) * 1024u + lane * 16;
    const unsigned so0 = (unsigned)(16 * wave) * 2752u + 64u * du0, ld0 = LB + (unsigned)(16 * wave) * 192u;
    auto hook = [&](int n) {
      if (n < 3) { dma16(rWG, v3g[n], so0, ld0 + n * 1024u); dma16(rWU, v3u[n], so0, ld0 + 24576u + n * 1024u); }
    };
    auto mid = [&]() {
      if (wave >= 4) {
        const int hw = wave - 4;
        poll_tag((lane == 4 || Hp < 0) ? (const GAS u64*)(ws + OFF_READY + (unsigned)G * 256u) : (const GAS u64*)(ws + OFF_VF + (unsigned)(Hp * 8 + c) * 128u) + (lane & 3), T);
        if (Hp >= 0) {
          dma_run4_sc1(rWS, lane * 16, OFF_V + (unsigned)((2 * Hp) * 8 + c) * 4096u, LB + L_V + (unsigned)(8 * hw) * 1024u);
          dma_run4_sc1(rWS, lane * 16, OFF_V + (unsigned)((2 * Hp + 1) * 8 + c) * 4096u, LB + L_V + (unsigned)(8 * hw + 4) * 1024u);
        }
      }
    };
    auto st = [&](int i) { STAMP(i); };
    u32x4 of;
    if (tt >= 8) of = attention<true>(ka, va, asfrag(qf), 0, e, r, h, wave >= 4, mid, hook, st);
    else of = attention<false>(ka, va, asfrag(qf), 8 - tt, e, r, h, wave >= 4, mid, hook, st);
    __builtin_amdgcn_raw_buffer_store_b128(of, rWS, OFF_O + (unsigned)(tt * 32 + 4 * c + 2 * hp + e) * 1024u + lane * 16, 0, 16);
  }
  STAMP(17);
  const unsigned trpm = (unsigned)((4 * h + trq) * 128 + trcb * 32 + trp * 8);

  if (wave < 4) {
    const int kh = hp;
    VMWAIT(0);
    const unsigned haveO = arrive(cntT, lane);
    RBAR();
    STAMP(8);
    u32x2 xres[4];
#pragma unroll
    for (int g = 0; g < 4; ++g)
      xres[g] = __builtin_amdgcn_raw_buffer_load_b64(rX, (unsigned)((32 * tt + r) * 1024 + (64 * c + 32 * kh + 8 * g + 4 * h) * 2), 0, 0);
    wait_cnt(cntT, haveO, 32u);
    STAMP(9);
    u32x4 of[16];
#pragma unroll
    for (int s = 0; s < 16; ++s) of[s] = __builtin_amdgcn_raw_buffer_load_b128(rWS, OFF_O + (unsigned)(tt * 32 + 16 * kh + s) * 1024u + lane * 16, 0, 16);
    const unsigned aA = LB + L_WO + (unsigned)kh * 32768u + trpm + (unsigned)((kh ^ (trq >> 1)) * 64);
    const unsigned aB = aA ^ 64u;
    f32x16 accA, accB;
#pragma unroll
    for (int i = 0; i < 16; ++i) { accA[i] = 0.f; accB[i] = 0.f; }
    pipe<32, 6>([&](auto I_) { constexpr int i = I_.value; const unsigned a = ((i & 1) ? aA : aB) + (i >> 1) * 2048; return trfrag(a, a + 1024); },
                [&](auto I_, const bf16x8& w) { constexpr int i = I_.value; if (i & 1) MFMAP(accA, w, asfrag(of[i >> 1])); else MFMAP(accB, w, asfrag(of[i >> 1])); },
                [&](auto) {});
    STAMP(18);
    RBAR();
#pragma unroll
    for (int g = 0; g < 4; ++g) { f32x4 v = {accB[4 * g], accB[4 * g + 1], accB[4 * g + 2], accB[4 * g + 3]}; *(LAS f32x4*)(LB + L_SCR + (unsigned)wave * 4096u + g * 1024 + lane * 16) = v; }
    BAR();
    unsigned hres[8];
    {
      float hv[16];
#pragma unroll
      for (int g = 0; g < 4; ++g) {
        const f32x4 v = *(LAS f32x4*)(LB + L_SCR + (unsigned)(wave ^ 1) * 4096u + g * 1024 + lane * 16);
        hres[2 * g] = pk2(accA[4 * g] + v[0] + blo(xres[g][0]), accA[4 * g + 1] + v[1] + bhi(xres[g][0]));
        hres[2 * g + 1] = pk2(accA[4 * g + 2] + v[2] + blo(xres[g][1]), accA[4 * g + 3] + v[3] + bhi(xres[g][1]));
      }
      const unsigned o = OFF_H + (unsigned)(tt * 32 + 4 * c + 2 * kh) * 1024u + lane * 16;
      u32x4 h0 = {hres[0], hres[1], hres[2], hres[3]}, h1 = {hres[4], hres[5], hres[6], hres[7]};
      __builtin_amdgcn_raw_buffer_store_b128(h0, rWS, o, 0, 16);
      __builtin_amdgcn_raw_buffer_store_b128(h1, rWS, o + 1024, 0, 16);
      float sum = 0.f;
#pragma unroll
      for (int i = 0; i < 8; ++i) { hv[2 * i] = blo(hres[i]); hv[2 * i + 1] = bhi(hres[i]); sum += hv[2 * i] + hv[2 * i + 1]; }
      const float mean = swap32_add(sum) * (1.f / 32.f);
      float m2 = 0.f;
#pragma unroll
      for (int i = 0; i < 16; ++i) { const float d = hv[i] - mean; m2 = fmaf(d, d, m2); }
      m2 = swap32_add(m2);
      u32x2 st = {__float_as_uint(mean), __float_as_uint(m2)};
      if (h == 0) __builtin_amdgcn_raw_buffer_store_b64(st, rWS, OFF_HS + (unsigned)((tt * 16 + 2 * c + kh) * 256 + r * 8), 0, 16);
    }
    STAMP(19);
    VMWAIT(0);
    RBAR();
    (void)arrive(cntT + 32, lane);
    STAMP(10);

    const int t4 = 4 * G + wave;
    GAS unsigned* cnt4 = (GAS unsigned*)(ws + OFF_CNT + (unsigned)t4 * 1024u);
    wait_cnt(cnt4 + 32, 0u, 16u);
    STAMP(11);
    const unsigned hb = OFF_H + (unsigned)t4 * 32768u + lane * 16;
    u32x4 hf[32];
#pragma unroll
    for (int s = 0; s < 12; ++s) hf[s] = __builtin_amdgcn_raw_buffer_load_b128(rWS, hb + s * 1024, 0, 16);
    u32x2 stp[8];
    const unsigned w3e = LB + (unsigned)(trcb * 24576 + (4 * h + trq) * 192 + trp * 8 + trcb * 32);
    const unsigned w3o = LB + (unsigned)(trcb * 24576 + (4 * h + trq) * 192 + trp * 8 - trcb * 32);
    unsigned w3e1 = w3e + 49152u, w3o1 = w3o + 49152u, w3e2 = w3e + 98304u, w3o2 = w3o + 98304u, csa = LB + L_SCR + (unsigned)r * 16u;
    OPQ(w3e1); OPQ(w3o1); OPQ(w3e2); OPQ(w3o2); OPQ(csa);
    f32x16 ac[6];
#pragma unroll
    for (int u = 0; u < 6; ++u)
#pragma unroll
      for (int i = 0; i < 16; ++i) ac[u][i] = 0.f;
    float rsg, irs;
    u32x4 mfr;
    float tg[8];
    const unsigned hho = OFF_HH + (unsigned)(t4 * 86 + 2 * du0) * 1024u + lane * 16;
    const unsigned hho2 = ndu == 3 ? hho + 4096u : 0x7F000000u;
    auto epi = [&](int u, int k) {
      if (k == 1) {
#pragma unroll
        for (int i = 0; i < 8; ++i) tg[i] = ac[u][i] * rsg;
      } else if (k == 2 || k == 3) {
#pragma unroll
        for (int i = 4 * (k - 2); i < 4 * (k - 1); ++i) tg[i] = __builtin_amdgcn_exp2f(tg[i]);
      } else if (k == 4) {
#pragma unroll
        for (int i = 0; i < 8; ++i) tg[i] = fmaf(tg[i], irs, irs);
      } else if (k == 5 || k == 6) {
#pragma unroll
        for (int i = 4 * (k - 5); i < 4 * (k - 4); ++i) tg[i] = __builtin_amdgcn_rcpf(tg[i]);
      } else if (k == 7) {
#pragma unroll
        for (int i = 0; i < 8; ++i) tg[i] *= ac[u][8 + i];
      } else if (k == 8) {
        u32x4 o = {pk2(tg[0], tg[1]), pk2(tg[2], tg[3]), pk2(tg[4], tg[5]), pk2(tg[6], tg[7])};
        __builtin_amdgcn_raw_buffer_store_b128(o, rWS, u < 4 ? hho + u * 1024 : hho2 + (u - 4) * 1024, 0, 16);
      }
      if (k >= 1 && k <= 7) {
#pragma unroll
        for (int i = 0; i < 8; ++i) OPQ(tg[i]);
      }
    };
    pipe<198, 6>(
        [&](auto I_) __attribute__((always_inline)) {
          constexpr int i = I_.value;
          if constexpr (i < 144) {
            constexpr int u = i % 6, s = (i % 48) / 6;
            const unsigned a = ((i / 48 == 1) ? ((u & 1) ? w3o1 : w3e1) : ((u & 1) ? w3o : w3e)) + s * 3072 + u * 32;
            return trfrag(a, a + 1536);
          } else {
            constexpr int u = (i - 144) / 9, s = (i - 144) % 9;
            if constexpr (s == 8) return ldsfrag(csa + u * 512);
            else { const unsigned a = ((u & 1) ? w3o2 : w3e2) + s * 3072 + u * 32; return trfrag(a, a + 1536); }
          }
        },
        [&](auto I_, const bf16x8& w) __attribute__((always_inline)) {
          constexpr int i = I_.value;
          if constexpr (i < 144) {
            constexpr int u = i % 6, ks = i / 6, s = ks < 16 ? ks : ks + 8;
            MFMAP(ac[u], w, asfrag(hf[s]));
            if constexpr (u == 5 && ks + 12 < 32) { constexpr int k2 = ks + 12, s2 = k2 < 16 ? k2 : k2 < 24 ? k2 + 8 : k2 - 8; hf[s2] = __builtin_amdgcn_raw_buffer_load_b128(rWS, hb + s2 * 1024, 0, 16); }
            if (i == 121) {
#pragma unroll
              for (int q = 0; q < 8; ++q) stp[q] = __builtin_amdgcn_raw_buffer_load_b64(rWS, OFF_HS + (unsigned)((t4 * 16 + 2 * q + h) * 256 + r * 8), 0, 16);
            }
          } else {
            constexpr int u = (i - 144) / 9, s = (i - 144) % 9;
            if constexpr (s == 8) MFMAP(ac[u], w, asfrag(mfr));
            else MFMAP(ac[u], w, asfrag(hf[16 + s]));
            if (i == 146) {
              float ms = 0.f, q = 0.f;
#pragma unroll
              for (int k = 0; k < 8; ++k) { ms += __uint_as_float(stp[k][0]); q += __uint_as_float(stp[k][1]); }
              const float mean = swap32_add(ms) * (1.f / 16.f);
#pragma unroll
              for (int k = 0; k < 8; ++k) { const float d = __uint_as_float(stp[k][0]) - mean; q = fmaf(32.f * d, d, q); }
              q = swap32_add(q) * (1.f / 512.f) + 1e-5f;
              const float rs = __builtin_amdgcn_rsqf(q);
              rsg = rs * -1.44269504088896f;
              irs = q * rs;
              const unsigned a1 = pk2(-mean, 0.f) & 0xFFFFu;
              const float r1 = -mean - blo(a1);
              const unsigned a2 = pk2(r1, 0.f) & 0xFFFFu;
              const unsigned a3 = pk2(r1 - blo(a2), 0.f) & 0xFFFFu;
              mfr[0] = h ? 0u : (a1 | (a2 << 16)); mfr[1] = h ? 0u : (a1 | (a3 << 16)); mfr[2] = h ? 0u : (a2 | (a1 << 16)); mfr[3] = 0u;
            }
            if constexpr (u > 0) epi(u - 1, s);
            if (i == 144 + 43) { VMWAIT(1); signal(cnt4 + 64, lane); }
          }
        },
        [&](auto I_) __attribute__((always_inline)) {
          constexpr int i = I_.value;
          if (i == 54) RBAR();
          if (i == 96) { STAMP(21); RBAR(); STAMP(22); }
          if (i == 150) { RBAR(); STAMP(23); }
        });
    STAMP(12);
#pragma unroll
    for (int k = 1; k < 9; ++k) epi(5, k);
    VMWAIT(2);
    signal(cnt4 + 96, lane);
    VMWAIT(1);
    if (ndu == 3) signal(cnt4 + 128, lane);
    RBAR();
    STAMP(13);

    const unsigned gb = OFF_HH + (unsigned)(tt * 86 + kh) * 1024u + lane * 16;
    u32x4 gf[43];
    wait_cnt(cntT + 64, 0u, 16u);
    STAMP(14);
#pragma unroll
    for (int m = 0; m < 16; ++m) gf[m] = __builtin_amdgcn_raw_buffer_load_b128(rWS, gb + Dm(m) * 2048, 0, 16);
    STAMP(24);
    RBAR();
    STAMP(25);
#pragma unroll
    for (int m = 16; m < 36; ++m) gf[m] = __builtin_amdgcn_raw_buffer_load_b128(rWS, gb + Dm(m) * 2048, 0, 16);
    const unsigned dA = LB + (unsigned)kh * 2048u + trpm + (unsigned)((kh ^ (trq >> 1)) * 64);
    const unsigned dB = dA ^ 64u;
    unsigned dA1 = dA + 49152u, dB1 = dB + 49152u, dA2 = dA + 98304u, dB2 = dB + 98304u;
    OPQ(dA1); OPQ(dB1); OPQ(dA2); OPQ(dB2);
#pragma unroll
    for (int i = 0; i < 16; ++i) { accA[i] = 0.f; accB[i] = 0.f; }
    pipe<86, 6>(
        [&](auto I_) __attribute__((always_inline)) {
          constexpr int i = I_.value;
          constexpr int m = i >> 1, p = m / 12, mm = m % 12;
          const unsigned a = ((i & 1) ? ((p == 0 || p == 3) ? dA1 : p == 1 ? dA : dA2) : ((p == 0 || p == 3) ? dB1 : p == 1 ? dB : dB2)) + mm * 4096;
          return trfrag(a, a + 1024);
        },
        [&](auto I_, const bf16x8& w) __attribute__((always_inline)) {
          constexpr int i = I_.value;
          if (i & 1) MFMAP(accA, w, asfrag(gf[i >> 1])); else MFMAP(accB, w, asfrag(gf[i >> 1]));
          if constexpr ((i & 1) && (i >> 1) < 7) gf[36 + (i >> 1)] = __builtin_amdgcn_raw_buffer_load_b128(rWS, gb + Dm(36 + (i >> 1)) * 2048, 0, 16);
        },
        [&](auto I_) __attribute__((always_inline)) {
          constexpr int i = I_.value;
          if (i == 30) RBAR();
          if (i == 48) RBAR();
          if (i == 72) RBAR();
        });
    STAMP(26);
#pragma unroll
    for (int g = 0; g < 4; ++g) { f32x4 v = {accB[4 * g], accB[4 * g + 1], accB[4 * g + 2], accB[4 * g + 3]}; *(LAS f32x4*)(LB + L_SCR + (unsigned)wave * 4096u + g * 1024 + lane * 16) = v; }
    BAR();
#pragma unroll
    for (int g = 0; g < 4; ++g) {
      const f32x4 v = *(LAS f32x4*)(LB + L_SCR + (unsigned)(wave ^ 1) * 4096u + g * 1024 + lane * 16);
      u32x2 o;
      o[0] = pk2(accA[4 * g] + v[0] + blo(hres[2 * g]), accA[4 * g + 1] + v[1] + bhi(hres[2 * g]));
      o[1] = pk2(accA[4 * g + 2] + v[2] + blo(hres[2 * g + 1]), accA[4 * g + 3] + v[3] + bhi(hres[2 * g + 1]));
      __builtin_amdgcn_raw_buffer_store_b64(o, rOUT, (unsigned)((32 * tt + r) * 1024 + (64 * c + 32 * kh + 8 * g + 4 * h) * 2), 0, 0);
    }
    STAMP(15);
  } else {
    const int hw = wave - 4;
    auto w3blk = [&](int blk, unsigned dst) {
#pragma unroll 1
      for (int p = 0; p < 2; ++p) {
        const unsigned row0 = (unsigned)(32 * hw + 16 * p), so = (128u * blk + row0) * 2752u + 64u * du0, ld = LB + dst + row0 * 192u;
#pragma unroll
        for (int i = 0; i < 3; ++i) {
          dma16(rWG, v3g[i], so, ld + i * 1024u);
          dma16(rWU, v3u[i], so, ld + 24576u + i * 1024u);
        }
      }
    };
    VMWAIT(0);
    signal(cntT, lane);
    RBAR();
    w3blk(1, 49152);
    u32x4 b3[12];
#pragma unroll
    for (int q = 0; q < 12; ++q) {
      const unsigned so = (384u + (unsigned)(32 * hw + 16 * (q / 6))) * 2752u + 64u * du0;
      if ((q % 6) < 3) asm volatile("buffer_load_dwordx4 %0, %1, %2, %3 offen" : "=v"(b3[q]) : "v"(v3g[q % 3]), "s"(rWG), "s"(so) : "memory");
      else asm volatile("buffer_load_dwordx4 %0, %1, %2, %3 offen" : "=v"(b3[q]) : "v"(v3u[q % 3]), "s"(rWU), "s"(so) : "memory");
    }
    VMWAIT(12);
    RBAR();
    RBAR();
    RBAR();
    w3blk(2, 98304);
    asm volatile("s_waitcnt vmcnt(0)" : "+v"(b3[0]), "+v"(b3[1]), "+v"(b3[2]), "+v"(b3[3]), "+v"(b3[4]), "+v"(b3[5]), "+v"(b3[6]), "+v"(b3[7]), "+v"(b3[8]), "+v"(b3[9]), "+v"(b3[10]), "+v"(b3[11])::"memory");
    if (hw == (G & 3)) {
      const int blk = G >> 2;
      if (blk == 3) {
#pragma unroll
        for (int q = 0; q < 12; ++q) ldsst(LB + L_SCR + (unsigned)(16 * (q / 6)) * 192u + ((q % 6) >= 3 ? 6144u : 0u) + (unsigned)(q % 3) * 1024u + lane * 16, b3[q]);
        asm volatile("s_waitcnt lgkmcnt(0)" ::: "memory");
      }
      const unsigned we = LB + (blk == 3 ? L_SCR + (unsigned)trcb * 6144u : (unsigned)(blk * 49152 + trcb * 24576 + 32 * hw * 192)) + (unsigned)((4 * h + trq) * 192 + trp * 8 + trcb * 32);
      const unsigned wo = we - (unsigned)(trcb * 64);
      f32x16 cs;
#pragma unroll
      for (int i = 0; i < 16; ++i) cs[i] = 0.f;
      bf16x8 f[12];
#pragma unroll
      for (int q = 0; q < 12; ++q) { const int u = q >> 1; const unsigned a = ((u & 1) ? wo : we) + (q & 1) * 3072 + u * 32; f[q] = trfrag(a, a + 1536); }
#pragma unroll
      for (int q = 0; q < 12; ++q) {
        const unsigned sel = (r == (q >> 1)) ? 0x3F803F80u : 0u;
        const u32x4 sv = {sel, sel, sel, sel};
        cs = MFMA(f[q], asfrag(sv), cs);
      }
      if (r < 6) {
        const unsigned tg32 = (unsigned)T;
#pragma unroll
        for (int g = 0; g < 16; ++g)
          __hip_atomic_store((GAS u64*)(ws + OFF_CS2 + (unsigned)((j * 16 + G) * 1536 + (r * 32 + (g >> 2) * 8 + h * 4 + (g & 3)) * 8)), ((u64)tg32 << 32) | __float_as_uint(cs[g]), RLX_AGENT);
      }
    }
    if (b == 0 && hw == 3) {
      unsigned spins = 0;
#pragma unroll 1
      for (;;) {
        bool ok = true;
#pragma unroll
        for (int i = 0; i < 4; ++i) ok &= __hip_atomic_load((const GAS u64*)(ws + OFF_KF + (unsigned)(i * 64 + lane) * 128u), RLX_AGENT) == T;
        if (__all(ok)) break;
        __builtin_amdgcn_s_sleep(2);
        if (++spins > SPIN_CAP) break;
      }
      if (lane == 0) __hip_atomic_store((GAS u64*)(ws + OFF_EPOCH), T, RLX_AGENT);
    }
    STAMP(20);
    RBAR();
#pragma unroll
    for (int q = 0; q < 12; ++q)
      ldsst(LB + (unsigned)(32 * hw + 16 * (q / 6)) * 192u + ((q % 6) >= 3 ? 24576u : 0u) + (unsigned)(q % 3) * 1024u + lane * 16, b3[q]);
    STAMP(21);
    BAR();
    if (hw < 3) {
      float pv[16];
      unsigned spins = 0;
#pragma unroll 1
      for (;;) {
        bool ok = true;
#pragma unroll
        for (int g = 0; g < 16; ++g) {
          const u64 x = __hip_atomic_load((const GAS u64*)(ws + OFF_CS2 + (unsigned)((j * 16 + g) * 1536 + (hw * 64 + lane) * 8)), RLX_AGENT);
          pv[g] = __uint_as_float((unsigned)x);
          ok &= (unsigned)(x >> 32) == (unsigned)T;
        }
        if (__all(ok)) break;
        __builtin_amdgcn_s_sleep(2);
        if (++spins > SPIN_CAP) break;
      }
      const float v = (((pv[0] + pv[1]) + (pv[2] + pv[3])) + ((pv[4] + pv[5]) + (pv[6] + pv[7]))) + (((pv[8] + pv[9]) + (pv[10] + pv[11])) + ((pv[12] + pv[13]) + (pv[14] + pv[15])));
      const unsigned c1 = pk2(v, 0.f) & 0xFFFFu;
      const float r1 = v - blo(c1);
      const unsigned c2 = pk2(r1, 0.f) & 0xFFFFu;
      const unsigned c3 = pk2(r1 - blo(c2), 0.f) & 0xFFFFu;
      const u32x4 f = {c1 | (c1 << 16), c2 | (c1 << 16), c2 | (c3 << 16), 0u};
      ldsst(LB + L_SCR + (unsigned)(hw * 64 + lane) * 16u, f);
    }
    BAR();
    auto w4 = [&](int m0, int n, unsigned dst) {
#pragma unroll 1
      for (int q = 0; q < n; ++q) {
        const int mm = hw + 4 * q;
        dma_run4(rWD, v1k, (unsigned)Dm(m0 + mm) * 32768u + 128u * c, 8192, LB + dst + (unsigned)mm * 4096u, 1);
      }
    };
    w4(0, 3, 49152);
    w4(12, 3, 0);
    RBAR();
    w4(24, 3, 98304);
    wait_cnt(cntT + 96 + 32 * hp, 0u, hp ? 11u : 16u);
    VMWAIT(0);
    RBAR();
    RBAR();
    w4(36, 2, 49152);
    VMWAIT(8);
    RBAR();
    VMWAIT(0);
    RBAR();
    RBAR();
  }
  STAMP_END();
}

extern "C" void kernel_launch(void* const* d_in, const int* in_sizes, int n_in, void* d_out, int out_size, void* d_ws, size_t ws_size,
                              hipStream_t stream) {
  (void)in_sizes; (void)n_in; (void)out_size; (void)ws_size;
  (void)hipFuncSetAttribute((const void*)fused_block, hipFuncAttributeMaxDynamicSharedMemorySize, (int)LDS_BYTES);
  hipLaunchKernelGGL(fused_block, dim3(256), dim3(512), LDS_BYTES, stream, d_in[0], d_in[1], d_in[2], d_in[4], d_in[5], d_in[6], d_out, (char*)d_ws);
}
```

```cpp
#include <hip/hip_runtime.h>
#include <hip/hip_bf16.h>
#include <utility>

typedef __bf16 bf16x8 __attribute__((ext_vector_type(8)));
typedef __bf16 bf16x4 __attribute__((ext_vector_type(4)));
typedef __bf16 bf16x2 __attribute__((ext_vector_type(2)));
typedef float f32x16 __attribute__((ext_vector_type(16)));
typedef float f32x4 __attribute__((ext_vector_type(4)));
typedef float f32x2 __attribute__((ext_vector_type(2)));
typedef unsigned u32x4 __attribute__((ext_vector_type(4)));
typedef unsigned u32x2 __attribute__((ext_vector_type(2)));
typedef unsigned long long u64;
#define LAS __attribute__((address_space(3)))
#define GAS __attribute__((address_space(1)))
#define DI __device__ __forceinline__
#define RLX_AGENT __ATOMIC_RELAXED, __HIP_MEMORY_SCOPE_AGENT
#define MFMA(a, b, c) __builtin_amdgcn_mfma_f32_32x32x16_bf16((a), (b), (c), 0, 0, 0)
#define MFMAP(acc, a, b) do { acc = MFMA(a, b, acc); asm volatile("" : "+v"(acc)); } while (0)
#define OPQ(x) asm volatile("" : "+v"(x))
typedef __amdgpu_buffer_rsrc_t rsrc_t;

constexpr unsigned OFF_EPOCH = 0;
constexpr unsigned OFF_READY = 4096;
constexpr unsigned OFF_KF = 16384;
constexpr unsigned OFF_VF = OFF_KF + 32768;
constexpr unsigned OFF_CNT = OFF_VF + 32768;
constexpr unsigned OFF_CSF = 192u << 10;
constexpr unsigned OFF_CS = 200u << 10;
constexpr unsigned OFF_CS2 = 9u * (1u << 20) + (512u << 10);
constexpr unsigned OFF_STAMP = 512u << 10;
constexpr unsigned OFF_K = 1u << 20;
constexpr unsigned OFF_V = 3u << 20;
constexpr unsigned OFF_O = 5u << 20;
constexpr unsigned OFF_H = 7u << 20;
constexpr unsigned OFF_HS = 9u << 20;
constexpr unsigned OFF_HH = 10u << 20;
constexpr unsigned WS_BYTES = 16u << 20;
constexpr unsigned KB = 1024;
constexpr unsigned L_WK = 0, L_WV = 64 * KB, L_XLN = 96 * KB, L_WQA = 128 * KB, L_WQB = 32 * KB;
constexpr unsigned L_K = 0, L_V = 48 * KB, L_QH = 40 * KB, L_ONES = 88 * KB, L_WO = 96 * KB;
constexpr unsigned L_SCR = 144 * KB;
constexpr unsigned LDS_BYTES = 160 * KB;
constexpr unsigned SPIN_CAP = 1u << 18;

#define BAR() do { asm volatile("s_waitcnt lgkmcnt(0)" ::: "memory"); __builtin_amdgcn_s_barrier(); asm volatile("" ::: "memory"); } while (0)
#define VMWAIT(n) asm volatile("s_waitcnt vmcnt(" #n ")" ::: "memory")

#define STAMP(i) do {} while (0)
#define STAMP_END() do {} while (0)

DI unsigned rfl(unsigned x) { return (unsigned)__builtin_amdgcn_readfirstlane((int)x); }
DI void dma16(rsrc_t rs, unsigned voff, unsigned soff, unsigned lds) {
  asm volatile("s_mov_b32 m0, %0\n\ts_nop 0\n\tbuffer_load_dwordx4 %1, %2, %3 offen lds" ::"s"(lds), "v"(voff), "s"(rs), "s"(soff) : "m0", "memory");
}
DI void dma16_sc1(rsrc_t rs, unsigned voff, unsigned soff, unsigned lds) {
  asm volatile("s_mov_b32 m0, %0\n\ts_nop 0\n\tbuffer_load_dwordx4 %1, %2, %3 offen sc1 lds" ::"s"(lds), "v"(voff), "s"(rs), "s"(soff) : "m0", "memory");
}
DI void dma_run4(rsrc_t rs, unsigned voff, unsigned soff, unsigned step, unsigned lds, int n4) {
#pragma unroll 1
  for (int i = 0; i < n4; ++i) {
    asm volatile(
        "s_mov_b32 m0, %[l]\n\ts_nop 0\n\tbuffer_load_dwordx4 %[v], %[r], %[s] offen lds\n\t"
        "s_add_u32 m0, m0, 0x400\n\ts_add_u32 %[s], %[s], %[st]\n\tbuffer_load_dwordx4 %[v], %[r], %[s] offen lds\n\t"
        "s_add_u32 m0, m0, 0x400\n\ts_add_u32 %[s], %[s], %[st]\n\tbuffer_load_dwordx4 %[v], %[r], %[s] offen lds\n\t"
        "s_add_u32 m0, m0, 0x400\n\ts_add_u32 %[s], %[s], %[st]\n\tbuffer_load_dwordx4 %[v], %[r], %[s] offen lds\n\t"
        "s_add_u32 %[s], %[s], %[st]\n\ts_add_u32 %[l], %[l], 0x1000"
        : [s] "+s"(soff), [l] "+s"(lds) : [v] "v"(voff), [r] "s"(rs), [st] "s"(step) : "m0", "scc", "memory");
  }
}
DI void dma_run4_sc1(rsrc_t rs, unsigned voff, unsigned soff, unsigned lds) {
  asm volatile(
      "s_mov_b32 m0, %[l]\n\ts_nop 0\n\tbuffer_load_dwordx4 %[v], %[r], %[s] offen sc1 lds\n\t"
      "buffer_load_dwordx4 %[v], %[r], %[s] offen offset:1024 sc1 lds\n\t"
      "buffer_load_dwordx4 %[v], %[r], %[s] offen offset:2048 sc1 lds\n\t"
      "buffer_load_dwordx4 %[v], %[r], %[s] offen offset:3072 sc1 lds"
      ::[s] "s"(soff), [l] "s"(lds), [v] "v"(voff), [r] "s"(rs) : "m0", "memory");
}
#define RBAR() do { asm volatile("" ::: "memory"); __builtin_amdgcn_s_barrier(); asm volatile("" ::: "memory"); } while (0)
#define SCHED() __builtin_amdgcn_sched_barrier(0)
template <int... I, class F> DI void sfor_(std::integer_sequence<int, I...>, F&& f) { (f(std::integral_constant<int, I>{}), ...); }
template <int N, class F> DI void sfor(F&& f) { sfor_(std::make_integer_sequence<int, N>{}, f); }
template <int N, int D, class LD, class MM, class PRE>
DI void pipe(LD&& ld, MM&& mm, PRE&& pre) {
  bf16x8 buf[D];
  sfor<D>([&](auto I) { pre(I); buf[I.value] = ld(I); });
  SCHED();
  sfor<N>([&](auto S) {
    constexpr int s = S.value;
    mm(S, buf[s % D]);
    if constexpr (s + D < N) { pre(std::integral_constant<int, s + D>{}); buf[s % D] = ld(std::integral_constant<int, s + D>{}); }
    SCHED();
  });
}
DI bf16x8 trfrag(unsigned a0, unsigned a1) {
  bf16x4 lo = __builtin_amdgcn_ds_read_tr16_b64_v4bf16((LAS bf16x4*)a0);
  bf16x4 hi = __builtin_amdgcn_ds_read_tr16_b64_v4bf16((LAS bf16x4*)a1);
  return __builtin_shufflevector(lo, hi, 0, 1, 2, 3, 4, 5, 6, 7);
}
DI bf16x8 ldsfrag(unsigned a) { return *(LAS bf16x8*)a; }
DI void ldsst(unsigned a, u32x4 v) { *(LAS u32x4*)a = v; }
DI unsigned pk2(float a, float b) { f32x2 v = {a, b}; return __builtin_bit_cast(unsigned, __builtin_convertvector(v, bf16x2)); }
DI u32x4 pack8(const f32x16& x, int o) { u32x4 r; r[0] = pk2(x[o], x[o + 1]); r[1] = pk2(x[o + 2], x[o + 3]); r[2] = pk2(x[o + 4], x[o + 5]); r[3] = pk2(x[o + 6], x[o + 7]); return r; }
DI float blo(unsigned u) { return __uint_as_float(u << 16); }
DI float bhi(unsigned u) { return __uint_as_float(u & 0xffff0000u); }
DI bf16x8 asfrag(u32x4 v) { return __builtin_bit_cast(bf16x8, v); }
DI float swap32_add(float a) {
  auto r = __builtin_amdgcn_permlane32_swap(__float_as_uint(a), __float_as_uint(a), false, false);
  return __uint_as_float(r[0]) + __uint_as_float(r[1]);
}
DI float swap32_max(float a) {
  auto r = __builtin_amdgcn_permlane32_swap(__float_as_uint(a), __float_as_uint(a), false, false);
  return fmaxf(__uint_as_float(r[0]), __uint_as_float(r[1]));
}
template <int CTRL> DI float dpp_add(float a) { return a + __uint_as_float(__builtin_amdgcn_update_dpp(0, __float_as_uint(a), CTRL, 0xf, 0xf, true)); }
DI float row16_sum(float a) { a = dpp_add<0x128>(a); a = dpp_add<0x124>(a); a = dpp_add<0x122>(a); a = dpp_add<0x121>(a); return a; }
DI void red8(const float (&v)[8], float& A, float& B) {
  float t[4];
#pragma unroll
  for (int i = 0; i < 4; ++i) {
    auto r = __builtin_amdgcn_permlane32_swap(__float_as_uint(v[2 * i]), __float_as_uint(v[2 * i + 1]), false, false);
    t[i] = __uint_as_float(r[0]) + __uint_as_float(r[1]);
  }
  auto r0 = __builtin_amdgcn_permlane16_swap(__float_as_uint(t[0]), __float_as_uint(t[1]), false, false);
  auto r1 = __builtin_amdgcn_permlane16_swap(__float_as_uint(t[2]), __float_as_uint(t[3]), false, false);
  A = row16_sum(__uint_as_float(r0[0]) + __uint_as_float(r0[1]));
  B = row16_sum(__uint_as_float(r1[0]) + __uint_as_float(r1[1]));
}
DI float rdl(float a, int l) { return __uint_as_float((unsigned)__builtin_amdgcn_readlane((int)__float_as_uint(a), l)); }
DI void bc8(float A, float B, float (&o)[8]) {
  o[0] = rdl(A, 0); o[2] = rdl(A, 16); o[1] = rdl(A, 32); o[3] = rdl(A, 48);
  o[4] = rdl(B, 0); o[6] = rdl(B, 16); o[5] = rdl(B, 32); o[7] = rdl(B, 48);
}
DI void wait_cnt(GAS unsigned* c, unsigned have, unsigned target) {
  unsigned spins = 0;
#pragma unroll 1
  while (have < target) {
    __builtin_amdgcn_s_sleep(1);
    have = rfl(__hip_atomic_load(c, RLX_AGENT));
    if (++spins > SPIN_CAP) break;
  }
  __builtin_amdgcn_fence(__ATOMIC_ACQUIRE, "wavefront");
}
DI unsigned arrive(GAS unsigned* c, int lane) {
  unsigned r = 0;
  if (lane == 0) r = __hip_atomic_fetch_add(c, 1u, RLX_AGENT) + 1u;
  r = rfl(r);
  asm volatile("" ::"s"(r));
  return r;
}

__host__ __device__ constexpr int Dm(int m) { return m < 32 ? ((43 * (m & 15)) >> 4) + (m >> 4) : (4 + 8 * ((m - 32) >> 1) + 3 * ((m - 32) & 1) < 42 ? 4 + 8 * ((m - 32) >> 1) + 3 * ((m - 32) & 1) : 42); }

template <bool FULL, class MID, class HOOK, class ST>
DI u32x4 attention(const unsigned ka, const unsigned va, const bf16x8 q, const int n0, const int e, const int r, const int h, const bool helper, MID&& mid, HOOK&& hook, ST&& stamp) {
  bf16x8 kf[9];
#pragma unroll
  for (int n = 0; n < 9; ++n) kf[n] = ldsfrag(ka + n * 4096);
  f32x16 zero;
#pragma unroll
  for (int i = 0; i < 16; ++i) zero[i] = 0.f;
  f32x16 sm = MFMA(kf[8], q, zero);
  f32x16 sa, sb;
  if (FULL) {
    sb = MFMA(kf[0], q, zero);
    sa = MFMA(kf[1], q, zero);
    OPQ(sa);
  } else {
#pragma unroll
    for (int i = 0; i < 16; ++i) sb[i] = -1e30f;
  }
#pragma unroll
  for (int i = 0; i < 16; ++i) { const int kl = (i & 3) + 8 * (i >> 2) + 4 * h; sm[i] = (kl <= r) ? sm[i] : sb[i]; }
  float mx = sm[0], mx2 = sm[1];
#pragma unroll
  for (int i = 2; i < 16; i += 2) mx = fmaxf(fmaxf(mx, sm[i]), sm[i + 1]);
  if (FULL) {
    SCHED();
#pragma unroll
    for (int n = 1; n < 8; ++n) {
      if (n & 1) {
        if (n < 7) { sb = MFMA(kf[n + 1], q, zero); OPQ(sb); }
        SCHED();
#pragma unroll
        for (int i = 0; i < 16; i += 4) { mx = fmaxf(fmaxf(mx, sa[i]), sa[i + 1]); mx2 = fmaxf(fmaxf(mx2, sa[i + 2]), sa[i + 3]); }
      } else {
        sa = MFMA(kf[n + 1], q, zero); OPQ(sa);
        SCHED();
#pragma unroll
        for (int i = 0; i < 16; i += 4) { mx = fmaxf(fmaxf(mx, sb[i]), sb[i + 1]); mx2 = fmaxf(fmaxf(mx2, sb[i + 2]), sb[i + 3]); }
      }
      SCHED();
      if (n == 3) mid();
    }
  } else {
    mid();
#pragma unroll
    for (int n = 1; n < 8; ++n) {
      if (n >= n0) {
        const f32x16 s = MFMA(kf[n], q, zero);
#pragma unroll
        for (int i = 0; i < 16; ++i) mx = fmaxf(mx, s[i]);
      }
    }
  }
  mx = swap32_max(fmaxf(mx, mx2));
  f32x16 negm;
#pragma unroll
  for (int i = 0; i < 16; ++i) negm[i] = -mx;
  if (FULL) { sa = MFMA(kf[1], q, negm); OPQ(sa); }
  u32x4 p0, p1, d0, d1;
  {
    f32x16 p;
#pragma unroll
    for (int i = 0; i < 16; ++i) p[i] = __builtin_amdgcn_exp2f(sm[i] - mx);
    p0 = pack8(p, 0); p1 = pack8(p, 8);
#pragma unroll
    for (int k = 0; k < 4; ++k) {
      const int i0 = 2 * k, i1 = 8 + 2 * k;
      const int ka0 = (i0 & 3) + 8 * (i0 >> 2) + 4 * h, ka1 = (i1 & 3) + 8 * (i1 >> 2) + 4 * h;
      const unsigned m0 = (ka0 <= r ? 0xFFFFu : 0u) | (ka0 + 1 <= r ? 0xFFFF0000u : 0u);
      const unsigned m1 = (ka1 <= r ? 0xFFFFu : 0u) | (ka1 + 1 <= r ? 0xFFFF0000u : 0u);
      d0[k] = p0[k] & m0; d1[k] = p1[k] & m1;
    }
    p0 ^= d0; p1 ^= d1;
  }
  OPQ(p0); OPQ(p1); OPQ(d0); OPQ(d1);
  stamp(16);
  if (helper) VMWAIT(0);
  RBAR();
  stamp(7);
  const bool ones = ((r >> 4) != e);
  unsigned vp = ones ? L_ONES : va;
  const unsigned i1k = ones ? 0u : 1024u, i3k = ones ? 0u : 3072u;
  f32x16 o = zero;
  if (FULL) {
    const unsigned v8 = vp + 32 * i1k;
    const bf16x8 w80 = ldsfrag(v8), w81 = ldsfrag(v8 + i1k);
    const bf16x8 w00 = ldsfrag(vp); vp += i1k;
    const bf16x8 w01 = ldsfrag(vp); vp += i3k;
    bf16x8 v0 = ldsfrag(vp); vp += i1k;
    bf16x8 v1 = ldsfrag(vp); vp += i3k;
    o = MFMA(w80, asfrag(d0), o);
    o = MFMA(w81, asfrag(d1), o);
    o = MFMA(w00, asfrag(p0), o);
    o = MFMA(w01, asfrag(p1), o);
    OPQ(o);
    SCHED();
#pragma unroll
    for (int n = 1; n < 8; ++n) {
      bf16x8 n0f, n1f;
      if (n < 7) {
        if (n & 1) { sb = MFMA(kf[n + 1], q, negm); OPQ(sb); } else { sa = MFMA(kf[n + 1], q, negm); OPQ(sa); }
        n0f = ldsfrag(vp); vp += i1k;
        n1f = ldsfrag(vp); vp += i3k;
        hook(n - 1);
      }
      SCHED();
      f32x16& s = (n & 1) ? sa : sb;
#pragma unroll
      for (int i = 0; i < 8; ++i) s[i] = __builtin_amdgcn_exp2f(s[i]);
      MFMAP(o, v0, asfrag(pack8(s, 0)));
      SCHED();
#pragma unroll
      for (int i = 8; i < 16; ++i) s[i] = __builtin_amdgcn_exp2f(s[i]);
      MFMAP(o, v1, asfrag(pack8(s, 8)));
      SCHED();
      v0 = n0f; v1 = n1f;
    }
  } else {
#pragma unroll
    for (int n = 0; n < 6; ++n) hook(n);
    o = MFMA(ldsfrag(vp + 32 * i1k), asfrag(d0), o);
    o = MFMA(ldsfrag(vp + 33 * i1k), asfrag(d1), o);
#pragma unroll
    for (int n = 1; n < 8; ++n) {
      if (n >= n0) {
        f32x16 p = MFMA(kf[n], q, negm);
#pragma unroll
        for (int i = 0; i < 16; ++i) p[i] = __builtin_amdgcn_exp2f(p[i]);
        o = MFMA(ldsfrag(vp + (4 * n) * i1k), asfrag(pack8(p, 0)), o);
        o = MFMA(ldsfrag(vp + (4 * n + 1) * i1k), asfrag(pack8(p, 8)), o);
      }
    }
  }
  u32x4 of;
  if (e == 0) {
    const float il = __builtin_amdgcn_rcpf(o[8]);
#pragma unroll
    for (int i = 0; i < 8; ++i) o[i] *= il;
    of = pack8(o, 0);
    OPQ(of);
  } else {
    const float il = __builtin_amdgcn_rcpf(o[0]);
#pragma unroll
    for (int i = 8; i < 16; ++i) o[i] *= il;
    of = pack8(o, 8);
    OPQ(of);
  }
  return of;
}

DI void ln8(const u32x4* xr, const unsigned wb, const int lane) {
  float s1[8], s2[8], rs[8], nm[8];
  const bf16x2 one2 = __builtin_bit_cast(bf16x2, 0x3F803F80u);
#pragma unroll
  for (int i = 0; i < 8; ++i) {
    float a = 0.f, q = 0.f;
#pragma unroll
    for (int k = 0; k < 4; ++k) {
      const unsigned xu = xr[i][k];
      const bf16x2 v = __builtin_bit_cast(bf16x2, xu);
      a = __builtin_amdgcn_fdot2_f32_bf16(v, one2, a, false);
      q = __builtin_amdgcn_fdot2_f32_bf16(v, v, q, false);
    }
    s1[i] = a; s2[i] = q;
  }
  float A1, B1, A2, B2;
  red8(s1, A1, B1);
  red8(s2, A2, B2);
  A1 *= (1.f / 512.f); B1 *= (1.f / 512.f);
  A2 = __builtin_amdgcn_rsqf(fmaxf(fmaf(-A1, A1, A2 * (1.f / 512.f)), 0.f) + 1e-5f);
  B2 = __builtin_amdgcn_rsqf(fmaxf(fmaf(-B1, B1, B2 * (1.f / 512.f)), 0.f) + 1e-5f);
  bc8(A2, B2, rs);
  bc8(-A1 * A2, -B1 * B2, nm);
#pragma unroll
  for (int i = 0; i < 8; ++i) {
    u32x4 o;
#pragma unroll
    for (int k = 0; k < 4; ++k) o[k] = pk2(fmaf(blo(xr[i][k]), rs[i], nm[i]), fmaf(bhi(xr[i][k]), rs[i], nm[i]));
    ldsst(wb + (unsigned)((i ^ (lane & 7)) << 4), o);
  }
}
DI void poll_tag(const GAS u64* p, const u64 T) {
  unsigned spins = 0;
#pragma unroll 1
  for (;;) {
    const u64 v = __hip_atomic_load(p, RLX_AGENT);
    if (__all(v == T)) break;
    __builtin_amdgcn_s_sleep(1);
    if (++spins > SPIN_CAP) break;
  }
  asm volatile("" ::: "memory");
}
DI void signal(GAS unsigned* c, int lane) { if (lane == 0) (void)__hip_atomic_fetch_add(c, 1u, RLX_AGENT); }

__global__ void __launch_bounds__(512, 2)
fused_block(const void* __restrict__ gx, const void* __restrict__ gwqkv, const void* __restrict__ gwo, const void* __restrict__ gwg,
            const void* __restrict__ gwu, const void* __restrict__ gwd, void* __restrict__ gout, char* ws) {
  extern __shared__ __attribute__((aligned(16))) char smem[];
  const int lane = threadIdx.x & 63;
  const int wave = (int)rfl(threadIdx.x >> 6);
  STAMP(0);
  const int b = blockIdx.x, xcd = b & 7, idx = b >> 3;
  const int G = xcd * 2 + (idx >> 4), j = idx & 15, t = j >> 3, c = j & 7, H = 2 * G + t;
  const int r = lane & 31, h = lane >> 5;
  constexpr unsigned LB = 0;
  asm volatile("" ::"v"(smem));
  const rsrc_t rX = __builtin_amdgcn_make_buffer_rsrc((void*)gx, 0, 2048 * 512 * 2, 0x00020000);
  const rsrc_t rWQKV = __builtin_amdgcn_make_buffer_rsrc((void*)gwqkv, 0, 512 * 1536 * 2, 0x00020000);
  const rsrc_t rWO = __builtin_amdgcn_make_buffer_rsrc((void*)gwo, 0, 512 * 512 * 2, 0x00020000);
  const rsrc_t rWG = __builtin_amdgcn_make_buffer_rsrc((void*)gwg, 0, 512 * 1376 * 2, 0x00020000);
  const rsrc_t rWU = __builtin_amdgcn_make_buffer_rsrc((void*)gwu, 0, 512 * 1376 * 2, 0x00020000);
  const rsrc_t rWD = __builtin_amdgcn_make_buffer_rsrc((void*)gwd, 0, 1376 * 512 * 2, 0x00020000);
  const rsrc_t rOUT = __builtin_amdgcn_make_buffer_rsrc(gout, 0, 2048 * 512 * 2, 0x00020000);
  const rsrc_t rWS = __builtin_amdgcn_make_buffer_rsrc((void*)ws, 0, WS_BYTES, 0x00020000);

  const unsigned chsw = (unsigned)(((lane & 7) ^ (((lane >> 4) & 1) << 2)) * 16);
  const unsigned v1k = (unsigned)(lane >> 3) * 1024u + chsw;
  const int trq = (lane & 15) >> 2, trp = lane & 3, trcb = (lane >> 4) & 1;
  const int tau = (wave & 3) >> 1, hp = wave & 1;
  const int tt = 2 * H + tau;
  GAS unsigned* cntT = (GAS unsigned*)(ws + OFF_CNT + (unsigned)tt * 1024u);
  u32x4 qf;
  u64 T;

  const unsigned vq = (unsigned)(lane >> 3) * 3072u + chsw;
  {
    asm volatile("s_load_dwordx2 %0, %1, 0x0" : "=s"(T) : "s"(ws));
    u32x4 xr[8];
    const unsigned vo = lane * 16, so = (unsigned)(64 * H + 8 * wave) * 1024u;
#pragma unroll
    for (int i = 0; i < 8; ++i)
      asm volatile("buffer_load_dwordx4 %0, %1, %2, %3 offen offset:%4" : "=v"(xr[i]) : "v"(vo + (i >> 2) * 4096), "s"(rX), "s"(so), "i"((i & 3) * 1024) : "memory");
    {
      const unsigned m0v = L_WK + (unsigned)wave * 8192u;
      if (wave == 6) {
        u64 pc;
        asm volatile("s_getpc_b64 %0" : "=s"(pc));
        const u64 a = (pc & ~127ull) + (u64)((idx * 9 + (lane < 9 ? lane : 0)) * 128);
        asm volatile("s_mov_b32 m0, %1\n\ts_nop 0\n\tglobal_load_lds_dword %0, off" ::"v"(a), "s"(m0v) : "m0", "memory");
      } else {
        const int n = wave == 0 ? 48 : wave < 4 ? 43 : wave == 4 ? 16 : wave == 5 ? 8 : 1;
        const int ln = lane < n ? lane : 0;
        const unsigned po = wave == 5 ? (unsigned)ln * (2u << 20) + (1u << 20) : wave == 7 ? (unsigned)(32 * tt) * 1024u : (unsigned)(b * n + ln) * 128u;
        const rsrc_t rp = wave == 0 ? rWQKV : wave == 1 ? rWG : wave == 2 ? rWU : wave == 3 ? rWD : wave == 4 ? rWO : wave == 5 ? rWS : rOUT;
        asm volatile("s_mov_b32 m0, %2\n\ts_nop 0\n\tbuffer_load_dword %0, %1, 0 offen lds" ::"v"(po), "s"(rp), "s"(m0v) : "m0", "memory");
      }
    }
#define PFN 1
    RBAR();
    dma_run4(rWQKV, vq, (unsigned)(64 * wave) * 3072u + (512u + 64u * c) * 2u, 8 * 3072, LB + L_WK + (unsigned)(64 * wave) * 128u, 2);
    asm volatile("s_waitcnt vmcnt(%9) lgkmcnt(0)" : "+v"(xr[0]), "+v"(xr[1]), "+v"(xr[2]), "+v"(xr[3]), "+v"(xr[4]), "+v"(xr[5]), "+v"(xr[6]), "+v"(xr[7]), "+s"(T) : "i"(8 + PFN) : "memory");
    T += 1ull;
    STAMP(1);
    ln8(xr, LB + L_XLN + (unsigned)(wave >> 2) * 32768u + (unsigned)(lane >> 1) * 1024u + (unsigned)(lane & 1) * 512u + (unsigned)(wave & 3) * 128u, lane);
    STAMP(30);
  }
  const int Hp = H - 8 + wave;

  if (wave < 4) {
    dma_run4(rWQKV, vq, (unsigned)(64 * wave) * 3072u + (1024u + 64u * c) * 2u, 8 * 3072, LB + L_WV + (unsigned)(64 * wave) * 128u, 2);
    VMWAIT(8);
    BAR();
    STAMP(2);
    const unsigned trn = LB + (unsigned)((8 * h + trq) * 128 + ((hp ^ (trq >> 1)) * 64) + trcb * 32 + trp * 8);
    unsigned trn2 = trn + 65536u, trn3 = trn + L_WQA;
    OPQ(trn2); OPQ(trn3);
    bf16x8 xf[32];
    const unsigned xb = LB + L_XLN + (unsigned)tau * 32768u + (unsigned)h * 512u;
    auto ldx = [&](int s) { xf[s] = ldsfrag(xb + (unsigned)s * 1024u + (unsigned)((r ^ ((2 * (s & 3) + h) & 7)) << 4)); };
    f32x16 aK, aV, aQ;
#pragma unroll
    for (int i = 0; i < 16; ++i) { aK[i] = 0.f; aV[i] = 0.f; aQ[i] = 0.f; }
    u32x4 kf0, kf1, vf0, vf1;
#pragma unroll
    for (int s = 0; s < 4; ++s) ldx(s);
    const unsigned kvo = (unsigned)((tt * 8 + c) * 4 + hp * 2) * 1024u + lane * 16;
    pipe<32, 4>([&](auto I_) __attribute__((always_inline)) { constexpr int i = I_.value; return trfrag(trn + i * 2048, trn + i * 2048 + 512); },
                [&](auto I_, const bf16x8& w) __attribute__((always_inline)) {
                  constexpr int i = I_.value;
                  MFMAP(aK, w, xf[i]);
                  if constexpr (i + 4 < 32) ldx(i + 4);
                },
                [&](auto) {});
    kf0 = pack8(aK, 0); kf1 = pack8(aK, 8);
    __builtin_amdgcn_raw_buffer_store_b128(kf0, rWS, OFF_K + kvo, 0, 16);
    __builtin_amdgcn_raw_buffer_store_b128(kf1, rWS, OFF_K + kvo + 1024, 0, 16);
    STAMP(3);
    VMWAIT(2);
    RBAR();
    STAMP(27);
    pipe<64, 5>(
        [&](auto I_) __attribute__((always_inline)) {
          constexpr int i = I_.value;
          const unsigned a = i < 32 ? trn2 + i * 2048 : i < 48 ? trn3 + (i - 32) * 2048 : trn + L_WQB + (i - 48) * 2048;
          return trfrag(a, a + 512);
        },
        [&](auto I_, const bf16x8& w) __attribute__((always_inline)) {
          constexpr int i = I_.value;
          if constexpr (i < 32) MFMAP(aV, xf[i], w);
          else MFMAP(aQ, w, xf[i - 32]);
          if (i == 26) {
            VMWAIT(0);
            if (lane == 0) __hip_atomic_store((GAS u64*)(ws + OFF_KF + (unsigned)(H * 8 + c) * 128u) + wave, T, RLX_AGENT);
          }
          if (i == 33) {
            vf0 = pack8(aV, 0); vf1 = pack8(aV, 8);
            __builtin_amdgcn_raw_buffer_store_b128(vf0, rWS, OFF_V + kvo, 0, 16);
            __builtin_amdgcn_raw_buffer_store_b128(vf1, rWS, OFF_V + kvo + 1024, 0, 16);
            STAMP(4);
          }
          if (i == 60) {
            VMWAIT(0);
            if (lane == 0) __hip_atomic_store((GAS u64*)(ws + OFF_VF + (unsigned)(H * 8 + c) * 128u) + wave, T, RLX_AGENT);
          }
        },
        [&](auto I_) __attribute__((always_inline)) {
          constexpr int i = I_.value;
          if (i == 16) RBAR();
          if (i == 32) RBAR();
        });
    STAMP(5);
    RBAR();
#pragma unroll
    for (int i = 0; i < 16; ++i) aQ[i] *= 0.25f * 1.44269504088896f;
    qf = pack8(aQ, 0);
    const u32x4 qf1 = pack8(aQ, 8);
    ldsst(LB + L_K + (unsigned)((8 + tau) * 4 + hp * 2) * 1024u + lane * 16, kf0);
    ldsst(LB + L_K + (unsigned)((8 + tau) * 4 + hp * 2 + 1) * 1024u + lane * 16, kf1);
    ldsst(LB + L_V + (unsigned)((8 + tau) * 4 + hp * 2) * 1024u + lane * 16, vf0);
    ldsst(LB + L_V + (unsigned)((8 + tau) * 4 + hp * 2 + 1) * 1024u + lane * 16, vf1);
    ldsst(LB + L_QH + (unsigned)wave * 1024u + lane * 16, qf1);
    { const u32x4 one4 = {0x3F803F80u, 0x3F803F80u, 0x3F803F80u, 0x3F803F80u}; ldsst(LB + L_ONES, one4); }
    dma_run4(rWO, v1k, (unsigned)(128 * wave) * 1024u + 128u * c, 8 * 1024, LB + L_WO + (unsigned)(128 * wave) * 128u, 4);
    BAR();
  } else {
    const int hw = wave - 4;
    VMWAIT(0);
    BAR();
    u32x4 st[24];
#pragma unroll
    for (int i = 0; i < 24; ++i) {
      const unsigned so = (unsigned)((i < 8 || i >= 16 ? 256 : 0) + 64 * hw + 8 * (i & 7)) * 3072u + ((i < 8 ? 1024u : 0u) + 64u * c) * 2u;
      asm volatile("buffer_load_dwordx4 %0, %1, %2, %3 offen" : "=v"(st[i]) : "v"(vq), "s"(rWQKV), "s"(so) : "memory");
    }
    STAMP(3);
    RBAR();
    asm volatile("s_waitcnt vmcnt(16)" : "+v"(st[0]), "+v"(st[1]), "+v"(st[2]), "+v"(st[3]), "+v"(st[4]), "+v"(st[5]), "+v"(st[6]), "+v"(st[7])::"memory");
#pragma unroll
    for (int i = 0; i < 8; ++i) ldsst(LB + L_WV + (unsigned)(256 + 64 * hw + 8 * i) * 128u + lane * 16, st[i]);
    BAR();
    asm volatile("s_waitcnt vmcnt(0)" : "+v"(st[8]), "+v"(st[9]), "+v"(st[10]), "+v"(st[11]), "+v"(st[12]), "+v"(st[13]), "+v"(st[14]), "+v"(st[15]),
                 "+v"(st[16]), "+v"(st[17]), "+v"(st[18]), "+v"(st[19]), "+v"(st[20]), "+v"(st[21]), "+v"(st[22]), "+v"(st[23])::"memory");
#pragma unroll
    for (int i = 0; i < 8; ++i) {
      ldsst(LB + L_WQA + (unsigned)(64 * hw + 8 * i) * 128u + lane * 16, st[8 + i]);
      ldsst(LB + L_WQB + (unsigned)(64 * hw + 8 * i) * 128u + lane * 16, st[16 + i]);
    }
    BAR();
    if (j == 0 && hw == 0) {
      if (lane < 32) __hip_atomic_store((GAS unsigned*)(ws + OFF_CNT + (unsigned)(32 * G + lane) * 128u), 0u, RLX_AGENT);
      VMWAIT(0);
      if (lane == 0) __hip_atomic_store((GAS u64*)(ws + OFF_READY + (unsigned)G * 256u), T, RLX_AGENT);
    }
    if (Hp >= 0) {
      poll_tag((const GAS u64*)(ws + OFF_KF + (unsigned)(Hp * 8 + c) * 128u) + (lane & 3), T);
      dma_run4_sc1(rWS, lane * 16, OFF_K + (unsigned)((2 * Hp) * 8 + c) * 4096u, LB + L_K + (unsigned)(8 * hw) * 1024u);
      dma_run4_sc1(rWS, lane * 16, OFF_K + (unsigned)((2 * Hp + 1) * 8 + c) * 4096u, LB + L_K + (unsigned)(8 * hw + 4) * 1024u);
    }
    STAMP(5);
    RBAR();
    VMWAIT(0);
    RBAR();
    qf = *(LAS u32x4*)(LB + L_QH + (unsigned)hw * 1024u + lane * 16);
  }
  STAMP(6);

  const int du0 = (43 * j) >> 4, ndu = ((43 * (j + 1)) >> 4) - du0;
  unsigned v3g[3], v3u[3];
#pragma unroll
  for (int i = 0; i < 3; ++i) {
    const unsigned qd = (unsigned)(i * 64 + lane), row = qd / 12u, ch = qd - row * 12u;
    v3g[i] = row * 2752u + ch * 16u;
    v3u[i] = row * 2752u + (ch ^ 2u) * 16u;
  }
  {
    const int e = wave >> 2;
    const unsigned ka = LB + L_K + (unsigned)(tau * 4 + hp * 2 + e) * 1024u + lane * 16;
    const unsigned va = LB + L_V + (unsigned)(tau * 4 + hp * 2) * 1024u + lane * 16;
    const unsigned so0 = (unsigned)(16 * wave) * 2752u + 64u * du0, ld0 = LB + (unsigned)(16 * wave) * 192u;
    auto hook = [&](int n) {
      if (n < 3) { dma16(rWG, v3g[n], so0, ld0 + n * 1024u); dma16(rWU, v3u[n], so0, ld0 + 24576u + n * 1024u); }
    };
    auto mid = [&]() {
      if (wave >= 4) {
        const int hw = wave - 4;
        poll_tag((lane == 4 || Hp < 0) ? (const GAS u64*)(ws + OFF_READY + (unsigned)G * 256u) : (const GAS u64*)(ws + OFF_VF + (unsigned)(Hp * 8 + c) * 128u) + (lane & 3), T);
        if (Hp >= 0) {
          dma_run4_sc1(rWS, lane * 16, OFF_V + (unsigned)((2 * Hp) * 8 + c) * 4096u, LB + L_V + (unsigned)(8 * hw) * 1024u);
          dma_run4_sc1(rWS, lane * 16, OFF_V + (unsigned)((2 * Hp + 1) * 8 + c) * 4096u, LB + L_V + (unsigned)(8 * hw + 4) * 1024u);
        }
      }
    };
    auto st = [&](int i) { STAMP(i); };
    u32x4 of;
    if (tt >= 8) of = attention<true>(ka, va, asfrag(qf), 0, e, r, h, wave >= 4, mid, hook, st);
    else of = attention<false>(ka, va, asfrag(qf), 8 - tt, e, r, h, wave >= 4, mid, hook, st);
    __builtin_amdgcn_raw_buffer_store_b128(of, rWS, OFF_O + (unsigned)(tt * 32 + 4 * c + 2 * hp + e) * 1024u + lane * 16, 0, 16);
  }
  STAMP(17);
  const unsigned trpm = (unsigned)((4 * h + trq) * 128 + trcb * 32 + trp * 8);

  if (wave < 4) {
    const int kh = hp;
    VMWAIT(0);
    const unsigned haveO = arrive(cntT, lane);
    RBAR();
    STAMP(8);
    u32x2 xres[4];
#pragma unroll
    for (int g = 0; g < 4; ++g)
      xres[g] = __builtin_amdgcn_raw_buffer_load_b64(rX, (unsigned)((32 * tt + r) * 1024 + (64 * c + 32 * kh + 8 * g + 4 * h) * 2), 0, 0);
    wait_cnt(cntT, haveO, 32u);
    STAMP(9);
    u32x4 of[16];
#pragma unroll
    for (int s = 0; s < 16; ++s) of[s] = __builtin_amdgcn_raw_buffer_load_b128(rWS, OFF_O + (unsigned)(tt * 32 + 16 * kh + s) * 1024u + lane * 16, 0, 16);
    const unsigned aA = LB + L_WO + (unsigned)kh * 32768u + trpm + (unsigned)((kh ^ (trq >> 1)) * 64);
    const unsigned aB = aA ^ 64u;
    f32x16 accA, accB;
#pragma unroll
    for (int i = 0; i < 16; ++i) { accA[i] = 0.f; accB[i] = 0.f; }
    pipe<32, 6>([&](auto I_) { constexpr int i = I_.value; const unsigned a = ((i & 1) ? aA : aB) + (i >> 1) * 2048; return trfrag(a, a + 1024); },
                [&](auto I_, const bf16x8& w) { constexpr int i = I_.value; if (i & 1) MFMAP(accA, w, asfrag(of[i >> 1])); else MFMAP(accB, w, asfrag(of[i >> 1])); },
                [&](auto) {});
    STAMP(18);
    RBAR();
#pragma unroll
    for (int g = 0; g < 4; ++g) { f32x4 v = {accB[4 * g], accB[4 * g + 1], accB[4 * g + 2], accB[4 * g + 3]}; *(LAS f32x4*)(LB + L_SCR + (unsigned)wave * 4096u + g * 1024 + lane * 16) = v; }
    BAR();
    unsigned hres[8];
    {
      float hv[16];
#pragma unroll
      for (int g = 0; g < 4; ++g) {
        const f32x4 v = *(LAS f32x4*)(LB + L_SCR + (unsigned)(wave ^ 1) * 4096u + g * 1024 + lane * 16);
        hres[2 * g] = pk2(accA[4 * g] + v[0] + blo(xres[g][0]), accA[4 * g + 1] + v[1] + bhi(xres[g][0]));
        hres[2 * g + 1] = pk2(accA[4 * g + 2] + v[2] + blo(xres[g][1]), accA[4 * g + 3] + v[3] + bhi(xres[g][1]));
      }
      const unsigned o = OFF_H + (unsigned)(tt * 32 + 4 * c + 2 * kh) * 1024u + lane * 16;
      u32x4 h0 = {hres[0], hres[1], hres[2], hres[3]}, h1 = {hres[4], hres[5], hres[6], hres[7]};
      __builtin_amdgcn_raw_buffer_store_b128(h0, rWS, o, 0, 16);
      __builtin_amdgcn_raw_buffer_store_b128(h1, rWS, o + 1024, 0, 16);
      float sum = 0.f;
#pragma unroll
      for (int i = 0; i < 8; ++i) { hv[2 * i] = blo(hres[i]); hv[2 * i + 1] = bhi(hres[i]); sum += hv[2 * i] + hv[2 * i + 1]; }
      const float mean = swap32_add(sum) * (1.f / 32.f);
      float m2 = 0.f;
#pragma unroll
      for (int i = 0; i < 16; ++i) { const float d = hv[i] - mean; m2 = fmaf(d, d, m2); }
      m2 = swap32_add(m2);
      u32x2 st = {__float_as_uint(mean), __float_as_uint(m2)};
      if (h == 0) __builtin_amdgcn_raw_buffer_store_b64(st, rWS, OFF_HS + (unsigned)((tt * 16 + 2 * c + kh) * 256 + r * 8), 0, 16);
    }
    STAMP(19);
    VMWAIT(0);
    RBAR();
    (void)arrive(cntT + 32, lane);
    STAMP(10);

    const int t4 = 4 * G + wave;
    GAS unsigned* cnt4 = (GAS unsigned*)(ws + OFF_CNT + (unsigned)t4 * 1024u);
    wait_cnt(cnt4 + 32, 0u, 16u);
    STAMP(11);
    const unsigned hb = OFF_H + (unsigned)t4 * 32768u + lane * 16;
    u32x4 hf[32];
#pragma unroll
    for (int s = 0; s < 12; ++s) hf[s] = __builtin_amdgcn_raw_buffer_load_b128(rWS, hb + s * 1024, 0, 16);
    u32x2 stp[8];
    const unsigned w3e = LB + (unsigned)(trcb * 24576 + (4 * h + trq) * 192 + trp * 8 + trcb * 32);
    const unsigned w3o = LB + (unsigned)(trcb * 24576 + (4 * h + trq) * 192 + trp * 8 - trcb * 32);
    unsigned w3e1 = w3e + 49152u, w3o1 = w3o + 49152u, w3e2 = w3e + 98304u, w3o2 = w3o + 98304u, csa = LB + L_SCR + (unsigned)r * 16u;
    OPQ(w3e1); OPQ(w3o1); OPQ(w3e2); OPQ(w3o2); OPQ(csa);
    f32x16 ac[6];
#pragma unroll
    for (int u = 0; u < 6; ++u)
#pragma unroll
      for (int i = 0; i < 16; ++i) ac[u][i] = 0.f;
    float rsg, irs;
    u32x4 mfr;
    float tg[8];
    const unsigned hho = OFF_HH + (unsigned)(t4 * 86 + 2 * du0) * 1024u + lane * 16;
    const unsigned hho2 = ndu == 3 ? hho + 4096u : 0x7F000000u;
    auto epi = [&](int u, int k) {
      if (k == 1) {
#pragma unroll
        for (int i = 0; i < 8; ++i) tg[i] = ac[u][i] * rsg;
      } else if (k == 2 || k == 3) {
#pragma unroll
        for (int i = 4 * (k - 2); i < 4 * (k - 1); ++i) tg[i] = __builtin_amdgcn_exp2f(tg[i]);
      } else if (k == 4) {
#pragma unroll
        for (int i = 0; i < 8; ++i) tg[i] = fmaf(tg[i], irs, irs);
      } else if (k == 5 || k == 6) {
#pragma unroll
        for (int i = 4 * (k - 5); i < 4 * (k - 4); ++i) tg[i] = __builtin_amdgcn_rcpf(tg[i]);
      } else if (k == 7) {
#pragma unroll
        for (int i = 0; i < 8; ++i) tg[i] *= ac[u][8 + i];
      } else if (k == 8) {
        u32x4 o = {pk2(tg[0], tg[1]), pk2(tg[2], tg[3]), pk2(tg[4], tg[5]), pk2(tg[6], tg[7])};
        __builtin_amdgcn_raw_buffer_store_b128(o, rWS, u < 4 ? hho + u * 1024 : hho2 + (u - 4) * 1024, 0, 16);
      }
      if (k >= 1 && k <= 7) {
#pragma unroll
        for (int i = 0; i < 8; ++i) OPQ(tg[i]);
      }
    };
    pipe<198, 6>(
        [&](auto I_) __attribute__((always_inline)) {
          constexpr int i = I_.value;
          if constexpr (i < 144) {
            constexpr int u = i % 6, s = (i % 48) / 6;
            const unsigned a = ((i / 48 == 1) ? ((u & 1) ? w3o1 : w3e1) : ((u & 1) ? w3o : w3e)) + s * 3072 + u * 32;
            return trfrag(a, a + 1536);
          } else {
            constexpr int u = (i - 144) / 9, s = (i - 144) % 9;
            if constexpr (s == 8) return ldsfrag(csa + u * 512);
            else { const unsigned a = ((u & 1) ? w3o2 : w3e2) + s * 3072 + u * 32; return trfrag(a, a + 1536); }
          }
        },
        [&](auto I_, const bf16x8& w) __attribute__((always_inline)) {
          constexpr int i = I_.value;
          if constexpr (i < 144) {
            constexpr int u = i % 6, ks = i / 6, s = ks < 16 ? ks : ks + 8;
            MFMAP(ac[u], w, asfrag(hf[s]));
            if constexpr (u == 5 && ks + 12 < 32) { constexpr int k2 = ks + 12, s2 = k2 < 16 ? k2 : k2 < 24 ? k2 + 8 : k2 - 8; hf[s2] = __builtin_amdgcn_raw_buffer_load_b128(rWS, hb + s2 * 1024, 0, 16); }
            if (i == 121) {
#pragma unroll
              for (int q = 0; q < 8; ++q) stp[q] = __builtin_amdgcn_raw_buffer_load_b64(rWS, OFF_HS + (unsigned)((t4 * 16 + 2 * q + h) * 256 + r * 8), 0, 16);
            }
          } else {
            constexpr int u = (i - 144) / 9, s = (i - 144) % 9;
            if constexpr (s == 8) MFMAP(ac[u], w, asfrag(mfr));
            else MFMAP(ac[u], w, asfrag(hf[16 + s]));
            if (i == 146) {
              float ms = 0.f, q = 0.f;
#pragma unroll
              for (int k = 0; k < 8; ++k) { ms += __uint_as_float(stp[k][0]); q += __uint_as_float(stp[k][1]); }
              const float mean = swap32_add(ms) * (1.f / 16.f);
#pragma unroll
              for (int k = 0; k < 8; ++k) { const float d = __uint_as_float(stp[k][0]) - mean; q = fmaf(32.f * d, d, q); }
              q = swap32_add(q) * (1.f / 512.f) + 1e-5f;
              const float rs = __builtin_amdgcn_rsqf(q);
              rsg = rs * -1.44269504088896f;
              irs = q * rs;
              const unsigned a1 = pk2(-mean, 0.f) & 0xFFFFu;
              const float r1 = -mean - blo(a1);
              const unsigned a2 = pk2(r1, 0.f) & 0xFFFFu;
              const unsigned a3 = pk2(r1 - blo(a2), 0.f) & 0xFFFFu;
              mfr[0] = h ? 0u : (a1 | (a2 << 16)); mfr[1] = h ? 0u : (a1 | (a3 << 16)); mfr[2] = h ? 0u : (a2 | (a1 << 16)); mfr[3] = 0u;
            }
            if constexpr (u > 0) epi(u - 1, s);
            if (i == 144 + 43) { VMWAIT(1); signal(cnt4 + 64, lane); }
          }
        },
        [&](auto I_) __attribute__((always_inline)) {
          constexpr int i = I_.value;
          if (i == 54) RBAR();
          if (i == 96) { STAMP(21); RBAR(); STAMP(22); }
          if (i == 150) { RBAR(); STAMP(23); }
        });
    STAMP(12);
#pragma unroll
    for (int k = 1; k < 9; ++k) epi(5, k);
    VMWAIT(2);
    signal(cnt4 + 96, lane);
    VMWAIT(1);
    if (ndu == 3) signal(cnt4 + 128, lane);
    RBAR();
    STAMP(13);

    const unsigned gb = OFF_HH + (unsigned)(tt * 86 + kh) * 1024u + lane * 16;
    u32x4 gf[43];
    wait_cnt(cntT + 64, 0u, 16u);
    STAMP(14);
#pragma unroll
    for (int m = 0; m < 16; ++m) gf[m] = __builtin_amdgcn_raw_buffer_load_b128(rWS, gb + Dm(m) * 2048, 0, 16);
    STAMP(24);
    RBAR();
    STAMP(25);
#pragma unroll
    for (int m = 16; m < 36; ++m) gf[m] = __builtin_amdgcn_raw_buffer_load_b128(rWS, gb + Dm(m) * 2048, 0, 16);
    const unsigned dA = LB + (unsigned)kh * 2048u + trpm + (unsigned)((kh ^ (trq >> 1)) * 64);
    const unsigned dB = dA ^ 64u;
    unsigned dA1 = dA + 49152u, dB1 = dB + 49152u, dA2 = dA + 98304u, dB2 = dB + 98304u;
    OPQ(dA1); OPQ(dB1); OPQ(dA2); OPQ(dB2);
#pragma unroll
    for (int i = 0; i < 16; ++i) { accA[i] = 0.f; accB[i] = 0.f; }
    pipe<86, 6>(
        [&](auto I_) __attribute__((always_inline)) {
          constexpr int i = I_.value;
          constexpr int m = i >> 1, p = m / 12, mm = m % 12;
          const unsigned a = ((i & 1) ? ((p == 0 || p == 3) ? dA1 : p == 1 ? dA : dA2) : ((p == 0 || p == 3) ? dB1 : p == 1 ? dB : dB2)) + mm * 4096;
          return trfrag(a, a + 1024);
        },
        [&](auto I_, const bf16x8& w) __attribute__((always_inline)) {
          constexpr int i = I_.value;
          if (i & 1) MFMAP(accA, w, asfrag(gf[i >> 1])); else MFMAP(accB, w, asfrag(gf[i >> 1]));
          if constexpr ((i & 1) && (i >> 1) < 7) gf[36 + (i >> 1)] = __builtin_amdgcn_raw_buffer_load_b128(rWS, gb + Dm(36 + (i >> 1)) * 2048, 0, 16);
        },
        [&](auto I_) __attribute__((always_inline)) {
          constexpr int i = I_.value;
          if (i == 30) RBAR();
          if (i == 48) RBAR();
          if (i == 72) RBAR();
        });
    STAMP(26);
#pragma unroll
    for (int g = 0; g < 4; ++g) { f32x4 v = {accB[4 * g], accB[4 * g + 1], accB[4 * g + 2], accB[4 * g + 3]}; *(LAS f32x4*)(LB + L_SCR + (unsigned)wave * 4096u + g * 1024 + lane * 16) = v; }
    BAR();
#pragma unroll
    for (int g = 0; g < 4; ++g) {
      const f32x4 v = *(LAS f32x4*)(LB + L_SCR + (unsigned)(wave ^ 1) * 4096u + g * 1024 + lane * 16);
      u32x2 o;
      o[0] = pk2(accA[4 * g] + v[0] + blo(hres[2 * g]), accA[4 * g + 1] + v[1] + bhi(hres[2 * g]));
      o[1] = pk2(accA[4 * g + 2] + v[2] + blo(hres[2 * g + 1]), accA[4 * g + 3] + v[3] + bhi(hres[2 * g + 1]));
      __builtin_amdgcn_raw_buffer_store_b64(o, rOUT, (unsigned)((32 * tt + r) * 1024 + (64 * c + 32 * kh + 8 * g + 4 * h) * 2), 0, 0);
    }
    STAMP(15);
  } else {
    const int hw = wave - 4;
    auto w3blk = [&](int blk, unsigned dst) {
#pragma unroll 1
      for (int p = 0; p < 2; ++p) {
        const unsigned row0 = (unsigned)(32 * hw + 16 * p), so = (128u * blk + row0) * 2752u + 64u * du0, ld = LB + dst + row0 * 192u;
#pragma unroll
        for (int i = 0; i < 3; ++i) {
          dma16(rWG, v3g[i], so, ld + i * 1024u);
          dma16(rWU, v3u[i], so, ld + 24576u + i * 1024u);
        }
      }
    };
    VMWAIT(0);
    signal(cntT, lane);
    RBAR();
    w3blk(1, 49152);
    u32x4 b3[12];
#pragma unroll
    for (int q = 0; q < 12; ++q) {
      const unsigned so = (384u + (unsigned)(32 * hw + 16 * (q / 6))) * 2752u + 64u * du0;
      if ((q % 6) < 3) asm volatile("buffer_load_dwordx4 %0, %1, %2, %3 offen" : "=v"(b3[q]) : "v"(v3g[q % 3]), "s"(rWG), "s"(so) : "memory");
      else asm volatile("buffer_load_dwordx4 %0, %1, %2, %3 offen" : "=v"(b3[q]) : "v"(v3u[q % 3]), "s"(rWU), "s"(so) : "memory");
    }
    VMWAIT(12);
    RBAR();
    RBAR();
    RBAR();
    w3blk(2, 98304);
    asm volatile("s_waitcnt vmcnt(0)" : "+v"(b3[0]), "+v"(b3[1]), "+v"(b3[2]), "+v"(b3[3]), "+v"(b3[4]), "+v"(b3[5]), "+v"(b3[6]), "+v"(b3[7]), "+v"(b3[8]), "+v"(b3[9]), "+v"(b3[10]), "+v"(b3[11])::"memory");
    if (hw == (G & 3)) {
      const int blk = G >> 2;
      if (blk == 3) {
#pragma unroll
        for (int q = 0; q < 12; ++q) ldsst(LB + L_SCR + (unsigned)(16 * (q / 6)) * 192u + ((q % 6) >= 3 ? 6144u : 0u) + (unsigned)(q % 3) * 1024u + lane * 16, b3[q]);
        asm volatile("s_waitcnt lgkmcnt(0)" ::: "memory");
      }
      const unsigned we = LB + (blk == 3 ? L_SCR + (unsigned)trcb * 6144u : (unsigned)(blk * 49152 + trcb * 24576 + 32 * hw * 192)) + (unsigned)((4 * h + trq) * 192 + trp * 8 + trcb * 32);
      const unsigned wo = we - (unsigned)(trcb * 64);
      f32x16 cs;
#pragma unroll
      for (int i = 0; i < 16; ++i) cs[i] = 0.f;
      bf16x8 f[12];
#pragma unroll
      for (int q = 0; q < 12; ++q) { const int u = q >> 1; const unsigned a = ((u & 1) ? wo : we) + (q & 1) * 3072 + u * 32; f[q] = trfrag(a, a + 1536); }
#pragma unroll
      for (int q = 0; q < 12; ++q) {
        const unsigned sel = (r == (q >> 1)) ? 0x3F803F80u : 0u;
        const u32x4 sv = {sel, sel, sel, sel};
        cs = MFMA(f[q], asfrag(sv), cs);
      }
      if (r < 6) {
        const unsigned tg32 = (unsigned)T;
#pragma unroll
        for (int g = 0; g < 16; ++g)
          __hip_atomic_store((GAS u64*)(ws + OFF_CS2 + (unsigned)((j * 16 + G) * 1536 + (r * 32 + (g >> 2) * 8 + h * 4 + (g & 3)) * 8)), ((u64)tg32 << 32) | __float_as_uint(cs[g]), RLX_AGENT);
      }
    }
    if (b == 0 && hw == 3) {
      unsigned spins = 0;
#pragma unroll 1
      for (;;) {
        bool ok = true;
#pragma unroll
        for (int i = 0; i < 4; ++i) ok &= __hip_atomic_load((const GAS u64*)(ws + OFF_KF + (unsigned)(i * 64 + lane) * 128u), RLX_AGENT) == T;
        if (__all(ok)) break;
        __builtin_amdgcn_s_sleep(2);
        if (++spins > SPIN_CAP) break;
      }
      if (lane == 0) __hip_atomic_store((GAS u64*)(ws + OFF_EPOCH), T, RLX_AGENT);
    }
    STAMP(20);
    RBAR();
#pragma unroll
    for (int q = 0; q < 12; ++q)
      ldsst(LB + (unsigned)(32 * hw + 16 * (q / 6)) * 192u + ((q % 6) >= 3 ? 24576u : 0u) + (unsigned)(q % 3) * 1024u + lane * 16, b3[q]);
    STAMP(21);
    BAR();
    if (hw < 3) {
      float pv[16];
      unsigned spins = 0;
#pragma unroll 1
      for (;;) {
        bool ok = true;
#pragma unroll
        for (int g = 0; g < 16; ++g) {
          const u64 x = __hip_atomic_load((const GAS u64*)(ws + OFF_CS2 + (unsigned)((j * 16 + g) * 1536 + (hw * 64 + lane) * 8)), RLX_AGENT);
          pv[g] = __uint_as_float((unsigned)x);
          ok &= (unsigned)(x >> 32) == (unsigned)T;
        }
        if (__all(ok)) break;
        __builtin_amdgcn_s_sleep(2);
        if (++spins > SPIN_CAP) break;
      }
      const float v = (((pv[0] + pv[1]) + (pv[2] + pv[3])) + ((pv[4] + pv[5]) + (pv[6] + pv[7]))) + (((pv[8] + pv[9]) + (pv[10] + pv[11])) + ((pv[12] + pv[13]) + (pv[14] + pv[15])));
      const unsigned c1 = pk2(v, 0.f) & 0xFFFFu;
      const float r1 = v - blo(c1);
      const unsigned c2 = pk2(r1, 0.f) & 0xFFFFu;
      const unsigned c3 = pk2(r1 - blo(c2), 0.f) & 0xFFFFu;
      const u32x4 f = {c1 | (c1 << 16), c2 | (c1 << 16), c2 | (c3 << 16), 0u};
      ldsst(LB + L_SCR + (unsigned)(hw * 64 + lane) * 16u, f);
    }
    BAR();
    auto w4 = [&](int m0, int n, unsigned dst) {
#pragma unroll 1
      for (int q = 0; q < n; ++q) {
        const int mm = hw + 4 * q;
        dma_run4(rWD, v1k, (unsigned)Dm(m0 + mm) * 32768u + 128u * c, 8192, LB + dst + (unsigned)mm * 4096u, 1);
      }
    };
    w4(0, 3, 49152);
    w4(12, 3, 0);
    RBAR();
    w4(24, 3, 98304);
    wait_cnt(cntT + 96 + 32 * hp, 0u, hp ? 11u : 16u);
    VMWAIT(0);
    RBAR();
    RBAR();
    w4(36, 2, 49152);
    VMWAIT(8);
    RBAR();
    VMWAIT(0);
    RBAR();
    RBAR();
  }
  STAMP_END();
}

extern "C" void kernel_launch(void* const* d_in, const int* in_sizes, int n_in, void* d_out, int out_size, void* d_ws, size_t ws_size,
                              hipStream_t stream) {
  (void)in_sizes; (void)n_in; (void)out_size; (void)ws_size;
  (void)hipFuncSetAttribute((const void*)fused_block, hipFuncAttributeMaxDynamicSharedMemorySize, (int)LDS_BYTES);
  hipLaunchKernelGGL(fused_block, dim3(256), dim3(512), LDS_BYTES, stream, d_in[0], d_in[1], d_in[2], d_in[4], d_in[5], d_in[6], d_out, (char*)d_ws);
}
```

```cpp
#include <hip/hip_runtime.h>
#include <hip/hip_bf16.h>
#include <utility>

typedef __bf16 bf16x8 __attribute__((ext_vector_type(8)));
typedef __bf16 bf16x4 __attribute__((ext_vector_type(4)));
typedef __bf16 bf16x2 __attribute__((ext_vector_type(2)));
typedef float f32x16 __attribute__((ext_vector_type(16)));
typedef float f32x4 __attribute__((ext_vector_type(4)));
typedef float f32x2 __attribute__((ext_vector_type(2)));
typedef unsigned u32x4 __attribute__((ext_vector_type(4)));
typedef unsigned u32x2 __attribute__((ext_vector_type(2)));
typedef unsigned long long u64;
#define LAS __attribute__((address_space(3)))
#define GAS __attribute__((address_space(1)))
#define DI __device__ __forceinline__
#define RLX_AGENT __ATOMIC_RELAXED, __HIP_MEMORY_SCOPE_AGENT
#define MFMA(a, b, c) __builtin_amdgcn_mfma_f32_32x32x16_bf16((a), (b), (c), 0, 0, 0)
#define MFMAP(acc, a, b) do { acc = MFMA(a, b, acc); asm volatile("" : "+v"(acc)); } while (0)
#define OPQ(x) asm volatile("" : "+v"(x))
typedef __amdgpu_buffer_rsrc_t rsrc_t;

constexpr unsigned OFF_EPOCH = 0;
constexpr unsigned OFF_READY = 4096;
constexpr unsigned OFF_KF = 16384;
constexpr unsigned OFF_VF = OFF_KF + 32768;
constexpr unsigned OFF_CNT = OFF_VF + 32768;
constexpr unsigned OFF_CSF = 192u << 10;
constexpr unsigned OFF_CS = 200u << 10;
constexpr unsigned OFF_CS2 = 9u * (1u << 20) + (512u << 10);
constexpr unsigned OFF_STAMP = 512u << 10;
constexpr unsigned OFF_K = 1u << 20;
constexpr unsigned OFF_V = 3u << 20;
constexpr unsigned OFF_O = 5u << 20;
constexpr unsigned OFF_H = 7u << 20;
constexpr unsigned OFF_HS = 9u << 20;
constexpr unsigned OFF_HH = 10u << 20;
constexpr unsigned WS_BYTES = 16u << 20;
constexpr unsigned KB = 1024;
constexpr unsigned L_WK = 0, L_WV = 64 * KB, L_XLN = 96 * KB, L_WQA = 128 * KB, L_WQB = 32 * KB;
constexpr unsigned L_K = 0, L_V = 48 * KB, L_QH = 40 * KB, L_ONES = 88 * KB, L_WO = 96 * KB;
constexpr unsigned L_SCR = 144 * KB;
constexpr unsigned LDS_BYTES = 160 * KB;
constexpr unsigned SPIN_CAP = 1u << 18;

#define BAR() do { asm volatile("s_waitcnt lgkmcnt(0)" ::: "memory"); __builtin_amdgcn_s_barrier(); asm volatile("" ::: "memory"); } while (0)
#define VMWAIT(n) asm volatile("s_waitcnt vmcnt(" #n ")" ::: "memory")

#define STAMP(i) do {} while (0)
#define STAMP_END() do {} while (0)

DI unsigned rfl(unsigned x) { return (unsigned)__builtin_amdgcn_readfirstlane((int)x); }
DI void dma16(rsrc_t rs, unsigned voff, unsigned soff, unsigned lds) {
  asm volatile("s_mov_b32 m0, %0\n\ts_nop 0\n\tbuffer_load_dwordx4 %1, %2, %3 offen lds" ::"s"(lds), "v"(voff), "s"(rs), "s"(soff) : "m0", "memory");
}
DI void dma16_sc1(rsrc_t rs, unsigned voff, unsigned soff, unsigned lds) {
  asm volatile("s_mov_b32 m0, %0\n\ts_nop 0\n\tbuffer_load_dwordx4 %1, %2, %3 offen sc1 lds" ::"s"(lds), "v"(voff), "s"(rs), "s"(soff) : "m0", "memory");
}
DI void dma_run4(rsrc_t rs, unsigned voff, unsigned soff, unsigned step, unsigned lds, int n4) {
#pragma unroll 1
  for (int i = 0; i < n4; ++i) {
    asm volatile(
        "s_mov_b32 m0, %[l]\n\ts_nop 0\n\tbuffer_load_dwordx4 %[v], %[r], %[s] offen lds\n\t"
        "s_add_u32 m0, m0, 0x400\n\ts_add_u32 %[s], %[s], %[st]\n\tbuffer_load_dwordx4 %[v], %[r], %[s] offen lds\n\t"
        "s_add_u32 m0, m0, 0x400\n\ts_add_u32 %[s], %[s], %[st]\n\tbuffer_load_dwordx4 %[v], %[r], %[s] offen lds\n\t"
        "s_add_u32 m0, m0, 0x400\n\ts_add_u32 %[s], %[s], %[st]\n\tbuffer_load_dwordx4 %[v], %[r], %[s] offen lds\n\t"
        "s_add_u32 %[s], %[s], %[st]\n\ts_add_u32 %[l], %[l], 0x1000"
        : [s] "+s"(soff), [l] "+s"(lds) : [v] "v"(voff), [r] "s"(rs), [st] "s"(step) : "m0", "scc", "memory");
  }
}
DI void dma_run4_sc1(rsrc_t rs, unsigned voff, unsigned soff, unsigned lds) {
  asm volatile(
      "s_mov_b32 m0, %[l]\n\ts_nop 0\n\tbuffer_load_dwordx4 %[v], %[r], %[s] offen sc1 lds\n\t"
      "buffer_load_dwordx4 %[v], %[r], %[s] offen offset:1024 sc1 lds\n\t"
      "buffer_load_dwordx4 %[v], %[r], %[s] offen offset:2048 sc1 lds\n\t"
      "buffer_load_dwordx4 %[v], %[r], %[s] offen offset:3072 sc1 lds"
      ::[s] "s"(soff), [l] "s"(lds), [v] "v"(voff), [r] "s"(rs) : "m0", "memory");
}
#define RBAR() do { asm volatile("" ::: "memory"); __builtin_amdgcn_s_barrier(); asm volatile("" ::: "memory"); } while (0)
#define SCHED() __builtin_amdgcn_sched_barrier(0)
template <int... I, class F> DI void sfor_(std::integer_sequence<int, I...>, F&& f) { (f(std::integral_constant<int, I>{}), ...); }
template <int N, class F> DI void sfor(F&& f) { sfor_(std::make_integer_sequence<int, N>{}, f); }
template <int N, int D, class LD, class MM, class PRE>
DI void pipe(LD&& ld, MM&& mm, PRE&& pre) {
  bf16x8 buf[D];
  sfor<D>([&](auto I) { pre(I); buf[I.value] = ld(I); });
  SCHED();
  sfor<N>([&](auto S) {
    constexpr int s = S.value;
    mm(S, buf[s % D]);
    if constexpr (s + D < N) { pre(std::integral_constant<int, s + D>{}); buf[s % D] = ld(std::integral_constant<int, s + D>{}); }
    SCHED();
  });
}
DI bf16x8 trfrag(unsigned a0, unsigned a1) {
  bf16x4 lo = __builtin_amdgcn_ds_read_tr16_b64_v4bf16((LAS bf16x4*)a0);
  bf16x4 hi = __builtin_amdgcn_ds_read_tr16_b64_v4bf16((LAS bf16x4*)a1);
  return __builtin_shufflevector(lo, hi, 0, 1, 2, 3, 4, 5, 6, 7);
}
DI bf16x8 ldsfrag(unsigned a) { return *(LAS bf16x8*)a; }
DI void ldsst(unsigned a, u32x4 v) { *(LAS u32x4*)a = v; }
DI unsigned pk2(float a, float b) { f32x2 v = {a, b}; return __builtin_bit_cast(unsigned, __builtin_convertvector(v, bf16x2)); }
DI u32x4 pack8(const f32x16& x, int o) { u32x4 r; r[0] = pk2(x[o], x[o + 1]); r[1] = pk2(x[o + 2], x[o + 3]); r[2] = pk2(x[o + 4], x[o + 5]); r[3] = pk2(x[o + 6], x[o + 7]); return r; }
DI float blo(unsigned u) { return __uint_as_float(u << 16); }
DI float bhi(unsigned u) { return __uint_as_float(u & 0xffff0000u); }
DI bf16x8 asfrag(u32x4 v) { return __builtin_bit_cast(bf16x8, v); }
DI float swap32_add(float a) {
  auto r = __builtin_amdgcn_permlane32_swap(__float_as_uint(a), __float_as_uint(a), false, false);
  return __uint_as_float(r[0]) + __uint_as_float(r[1]);
}
DI float swap32_max(float a) {
  auto r = __builtin_amdgcn_permlane32_swap(__float_as_uint(a), __float_as_uint(a), false, false);
  return fmaxf(__uint_as_float(r[0]), __uint_as_float(r[1]));
}
template <int CTRL> DI float dpp_add(float a) { return a + __uint_as_float(__builtin_amdgcn_update_dpp(0, __float_as_uint(a), CTRL, 0xf, 0xf, true)); }
DI float row16_sum(float a) { a = dpp_add<0x128>(a); a = dpp_add<0x124>(a); a = dpp_add<0x122>(a); a = dpp_add<0x121>(a); return a; }
DI void red8(const float (&v)[8], float& A, float& B) {
  float t[4];
#pragma unroll
  for (int i = 0; i < 4; ++i) {
    auto r = __builtin_amdgcn_permlane32_swap(__float_as_uint(v[2 * i]), __float_as_uint(v[2 * i + 1]), false, false);
    t[i] = __uint_as_float(r[0]) + __uint_as_float(r[1]);
  }
  auto r0 = __builtin_amdgcn_permlane16_swap(__float_as_uint(t[0]), __float_as_uint(t[1]), false, false);
  auto r1 = __builtin_amdgcn_permlane16_swap(__float_as_uint(t[2]), __float_as_uint(t[3]), false, false);
  A = row16_sum(__uint_as_float(r0[0]) + __uint_as_float(r0[1]));
  B = row16_sum(__uint_as_float(r1[0]) + __uint_as_float(r1[1]));
}
DI float rdl(float a, int l) { return __uint_as_float((unsigned)__builtin_amdgcn_readlane((int)__float_as_uint(a), l)); }
DI void bc8(float A, float B, float (&o)[8]) {
  o[0] = rdl(A, 0); o[2] = rdl(A, 16); o[1] = rdl(A, 32); o[3] = rdl(A, 48);
  o[4] = rdl(B, 0); o[6] = rdl(B, 16); o[5] = rdl(B, 32); o[7] = rdl(B, 48);
}
DI void wait_cnt(GAS unsigned* c, unsigned have, unsigned target) {
  unsigned spins = 0;
#pragma unroll 1
  while (have < target) {
    __builtin_amdgcn_s_sleep(1);
    have = rfl(__hip_atomic_load(c, RLX_AGENT));
    if (++spins > SPIN_CAP) break;
  }
  __builtin_amdgcn_fence(__ATOMIC_ACQUIRE, "wavefront");
}
DI unsigned arrive(GAS unsigned* c, int lane) {
  unsigned r = 0;
  if (lane == 0) r = __hip_atomic_fetch_add(c, 1u, RLX_AGENT) + 1u;
  r = rfl(r);
  asm volatile("" ::"s"(r));
  return r;
}

__host__ __device__ constexpr int Dm(int m) { return m < 32 ? ((43 * (m & 15)) >> 4) + (m >> 4) : (4 + 8 * ((m - 32) >> 1) + 3 * ((m - 32) & 1) < 42 ? 4 + 8 * ((m - 32) >> 1) + 3 * ((m - 32) & 1) : 42); }

template <bool FULL, class MID, class HOOK, class ST>
DI u32x4 attention(const unsigned ka, const unsigned va, const bf16x8 q, const int n0, const int e, const int r, const int h, const bool helper, MID&& mid, HOOK&& hook, ST&& stamp) {
  bf16x8 kf[9];
#pragma unroll
  for (int n = 0; n < 9; ++n) kf[n] = ldsfrag(ka + n * 4096);
  f32x16 zero;
#pragma unroll
  for (int i = 0; i < 16; ++i) zero[i] = 0.f;
  f32x16 sm = MFMA(kf[8], q, zero);
  f32x16 sa, sb;
  if (FULL) {
    sb = MFMA(kf[0], q, zero);
    sa = MFMA(kf[1], q, zero);
    OPQ(sa);
  } else {
#pragma unroll
    for (int i = 0; i < 16; ++i) sb[i] = -1e30f;
  }
#pragma unroll
  for (int i = 0; i < 16; ++i) { const int kl = (i & 3) + 8 * (i >> 2) + 4 * h; sm[i] = (kl <= r) ? sm[i] : sb[i]; }
  float mx = sm[0], mx2 = sm[1];
#pragma unroll
  for (int i = 2; i < 16; i += 2) mx = fmaxf(fmaxf(mx, sm[i]), sm[i + 1]);
  if (FULL) {
    SCHED();
#pragma unroll
    for (int n = 1; n < 8; ++n) {
      if (n & 1) {
        if (n < 7) { sb = MFMA(kf[n + 1], q, zero); OPQ(sb); }
        SCHED();
#pragma unroll
        for (int i = 0; i < 16; i += 4) { mx = fmaxf(fmaxf(mx, sa[i]), sa[i + 1]); mx2 = fmaxf(fmaxf(mx2, sa[i + 2]), sa[i + 3]); }
      } else {
        sa = MFMA(kf[n + 1], q, zero); OPQ(sa);
        SCHED();
#pragma unroll
        for (int i = 0; i < 16; i += 4) { mx = fmaxf(fmaxf(mx, sb[i]), sb[i + 1]); mx2 = fmaxf(fmaxf(mx2, sb[i + 2]), sb[i + 3]); }
      }
      SCHED();
      if (n == 3) mid();
    }
  } else {
    mid();
#pragma unroll
    for (int n = 1; n < 8; ++n) {
      if (n >= n0) {
        const f32x16 s = MFMA(kf[n], q, zero);
#pragma unroll
        for (int i = 0; i < 16; ++i) mx = fmaxf(mx, s[i]);
      }
    }
  }
  mx = swap32_max(fmaxf(mx, mx2));
  f32x16 negm;
#pragma unroll
  for (int i = 0; i < 16; ++i) negm[i] = -mx;
  if (FULL) { sa = MFMA(kf[1], q, negm); OPQ(sa); }
  u32x4 p0, p1, d0, d1;
  {
    f32x16 p;
#pragma unroll
    for (int i = 0; i < 16; ++i) p[i] = __builtin_amdgcn_exp2f(sm[i] - mx);
    p0 = pack8(p, 0); p1 = pack8(p, 8);
#pragma unroll
    for (int k = 0; k < 4; ++k) {
      const int i0 = 2 * k, i1 = 8 + 2 * k;
      const int ka0 = (i0 & 3) + 8 * (i0 >> 2) + 4 * h, ka1 = (i1 & 3) + 8 * (i1 >> 2) + 4 * h;
      const unsigned m0 = (ka0 <= r ? 0xFFFFu : 0u) | (ka0 + 1 <= r ? 0xFFFF0000u : 0u);
      const unsigned m1 = (ka1 <= r ? 0xFFFFu : 0u) | (ka1 + 1 <= r ? 0xFFFF0000u : 0u);
      d0[k] = p0[k] & m0; d1[k] = p1[k] & m1;
    }
    p0 ^= d0; p1 ^= d1;
  }
  OPQ(p0); OPQ(p1); OPQ(d0); OPQ(d1);
  stamp(16);
  if (helper) VMWAIT(0);
  RBAR();
  stamp(7);
  const bool ones = ((r >> 4) != e);
  unsigned vp = ones ? L_ONES : va;
  const unsigned i1k = ones ? 0u : 1024u, i3k = ones ? 0u : 3072u;
  f32x16 o = zero;
  if (FULL) {
    const unsigned v8 = vp + 32 * i1k;
    const bf16x8 w80 = ldsfrag(v8), w81 = ldsfrag(v8 + i1k);
    const bf16x8 w00 = ldsfrag(vp); vp += i1k;
    const bf16x8 w01 = ldsfrag(vp); vp += i3k;
    bf16x8 v0 = ldsfrag(vp); vp += i1k;
    bf16x8 v1 = ldsfrag(vp); vp += i3k;
    o = MFMA(w80, asfrag(d0), o);
    o = MFMA(w81, asfrag(d1), o);
    o = MFMA(w00, asfrag(p0), o);
    o = MFMA(w01, asfrag(p1), o);
    OPQ(o);
    SCHED();
#pragma unroll
    for (int n = 1; n < 8; ++n) {
      bf16x8 n0f, n1f;
      if (n < 7) {
        if (n & 1) { sb = MFMA(kf[n + 1], q, negm); OPQ(sb); } else { sa = MFMA(kf[n + 1], q, negm); OPQ(sa); }
        n0f = ldsfrag(vp); vp += i1k;
        n1f = ldsfrag(vp); vp += i3k;
        hook(n - 1);
      }
      SCHED();
      f32x16& s = (n & 1) ? sa : sb;
#pragma unroll
      for (int i = 0; i < 8; ++i) s[i] = __builtin_amdgcn_exp2f(s[i]);
      MFMAP(o, v0, asfrag(pack8(s, 0)));
      SCHED();
#pragma unroll
      for (int i = 8; i < 16; ++i) s[i] = __builtin_amdgcn_exp2f(s[i]);
      MFMAP(o, v1, asfrag(pack8(s, 8)));
      SCHED();
      v0 = n0f; v1 = n1f;
    }
  } else {
#pragma unroll
    for (int n = 0; n < 6; ++n) hook(n);
    o = MFMA(ldsfrag(vp + 32 * i1k), asfrag(d0), o);
    o = MFMA(ldsfrag(vp + 33 * i1k), asfrag(d1), o);
#pragma unroll
    for (int n = 1; n < 8; ++n) {
      if (n >= n0) {
        f32x16 p = MFMA(kf[n], q, negm);
#pragma unroll
        for (int i = 0; i < 16; ++i) p[i] = __builtin_amdgcn_exp2f(p[i]);
        o = MFMA(ldsfrag(vp + (4 * n) * i1k), asfrag(pack8(p, 0)), o);
        o = MFMA(ldsfrag(vp + (4 * n + 1) * i1k), asfrag(pack8(p, 8)), o);
      }
    }
  }
  u32x4 of;
  if (e == 0) {
    const float il = __builtin_amdgcn_rcpf(o[8]);
#pragma unroll
    for (int i = 0; i < 8; ++i) o[i] *= il;
    of = pack8(o, 0);
    OPQ(of);
  } else {
    const float il = __builtin_amdgcn_rcpf(o[0]);
#pragma unroll
    for (int i = 8; i < 16; ++i) o[i] *= il;
    of = pack8(o, 8);
    OPQ(of);
  }
  return of;
}

DI void ln8(const u32x4* xr, const unsigned wb, const int lane) {
  float s1[8], s2[8], rs[8], nm[8];
  const bf16x2 one2 = __builtin_bit_cast(bf16x2, 0x3F803F80u);
#pragma unroll
  for (int i = 0; i < 8; ++i) {
    float a = 0.f, q = 0.f;
#pragma unroll
    for (int k = 0; k < 4; ++k) {
      const unsigned xu = xr[i][k];
      const bf16x2 v = __builtin_bit_cast(bf16x2, xu);
      a = __builtin_amdgcn_fdot2_f32_bf16(v, one2, a, false);
      q = __builtin_amdgcn_fdot2_f32_bf16(v, v, q, false);
    }
    s1[i] = a; s2[i] = q;
  }
  float A1, B1, A2, B2;
  red8(s1, A1, B1);
  red8(s2, A2, B2);
  A1 *= (1.f / 512.f); B1 *= (1.f / 512.f);
  A2 = __builtin_amdgcn_rsqf(fmaxf(fmaf(-A1, A1, A2 * (1.f / 512.f)), 0.f) + 1e-5f);
  B2 = __builtin_amdgcn_rsqf(fmaxf(fmaf(-B1, B1, B2 * (1.f / 512.f)), 0.f) + 1e-5f);
  bc8(A2, B2, rs);
  bc8(-A1 * A2, -B1 * B2, nm);
#pragma unroll
  for (int i = 0; i < 8; ++i) {
    u32x4 o;
#pragma unroll
    for (int k = 0; k < 4; ++k) o[k] = pk2(fmaf(blo(xr[i][k]), rs[i], nm[i]), fmaf(bhi(xr[i][k]), rs[i], nm[i]));
    ldsst(wb + (unsigned)((i ^ (lane & 7)) << 4), o);
  }
}
DI void poll_tag(const GAS u64* p, const u64 T) {
  unsigned spins = 0;
#pragma unroll 1
  for (;;) {
    const u64 v = __hip_atomic_load(p, RLX_AGENT);
    if (__all(v == T)) break;
    __builtin_amdgcn_s_sleep(1);
    if (++spins > SPIN_CAP) break;
  }
  asm volatile("" ::: "memory");
}
DI void signal(GAS unsigned* c, int lane) { if (lane == 0) (void)__hip_atomic_fetch_add(c, 1u, RLX_AGENT); }

__global__ void __launch_bounds__(512, 2)
fused_block(const void* __restrict__ gx, const void* __restrict__ gwqkv, const void* __restrict__ gwo, const void* __restrict__ gwg,
            const void* __restrict__ gwu, const void* __restrict__ gwd, void* __restrict__ gout, char* ws) {
  extern __shared__ __attribute__((aligned(16))) char smem[];
  const int lane = threadIdx.x & 63;
  const int wave = (int)rfl(threadIdx.x >> 6);
  STAMP(0);
  const int b = blockIdx.x, xcd = b & 7, idx = b >> 3;
  const int G = xcd * 2 + (idx >> 4), j = idx & 15, t = j >> 3, c = j & 7, H = 2 * G + t;
  const int r = lane & 31, h = lane >> 5;
  constexpr unsigned LB = 0;
  asm volatile("" ::"v"(smem));
  const rsrc_t rX = __builtin_amdgcn_make_buffer_rsrc((void*)gx, 0, 2048 * 512 * 2, 0x00020000);
  const rsrc_t rWQKV = __builtin_amdgcn_make_buffer_rsrc((void*)gwqkv, 0, 512 * 1536 * 2, 0x00020000);
  const rsrc_t rWO = __builtin_amdgcn_make_buffer_rsrc((void*)gwo, 0, 512 * 512 * 2, 0x00020000);
  const rsrc_t rWG = __builtin_amdgcn_make_buffer_rsrc((void*)gwg, 0, 512 * 1376 * 2, 0x00020000);
  const rsrc_t rWU = __builtin_amdgcn_make_buffer_rsrc((void*)gwu, 0, 512 * 1376 * 2, 0x00020000);
  const rsrc_t rWD = __builtin_amdgcn_make_buffer_rsrc((void*)gwd, 0, 1376 * 512 * 2, 0x00020000);
  const rsrc_t rOUT = __builtin_amdgcn_make_buffer_rsrc(gout, 0, 2048 * 512 * 2, 0x00020000);
  const rsrc_t rWS = __builtin_amdgcn_make_buffer_rsrc((void*)ws, 0, WS_BYTES, 0x00020000);

  const unsigned chsw = (unsigned)(((lane & 7) ^ (((lane >> 4) & 1) << 2)) * 16);
  const unsigned v1k = (unsigned)(lane >> 3) * 1024u + chsw;
  const int trq = (lane & 15) >> 2, trp = lane & 3, trcb = (lane >> 4) & 1;
  const int tau = (wave & 3) >> 1, hp = wave & 1;
  const int tt = 2 * H + tau;
  GAS unsigned* cntT = (GAS unsigned*)(ws + OFF_CNT + (unsigned)tt * 1024u);
  u32x4 qf;
  u64 T;

  const unsigned vq = (unsigned)(lane >> 3) * 3072u + chsw;
  {
    asm volatile("s_load_dwordx2 %0, %1, 0x0" : "=s"(T) : "s"(ws));
    u32x4 xr[8];
    const unsigned vo = lane * 16, so = (unsigned)(64 * H + 8 * wave) * 1024u;
#pragma unroll
    for (int i = 0; i < 8; ++i)
      asm volatile("buffer_load_dwordx4 %0, %1, %2, %3 offen offset:%4" : "=v"(xr[i]) : "v"(vo + (i >> 2) * 4096), "s"(rX), "s"(so), "i"((i & 3) * 1024) : "memory");
    {
      const unsigned m0v = L_WK + (unsigned)wave * 8192u;
      if (wave == 6) {
        u64 pc;
        asm volatile("s_getpc_b64 %0" : "=s"(pc));
        const u64 a = (pc & ~127ull) + (u64)((idx * 9 + (lane < 9 ? lane : 0)) * 128);
        asm volatile("s_mov_b32 m0, %1\n\ts_nop 0\n\tglobal_load_lds_dword %0, off" ::"v"(a), "s"(m0v) : "m0", "memory");
      } else {
        const int n = wave == 0 ? 48 : wave == 5 ? 8 : 1;
        const int ln = lane < n ? lane : 0;
        const unsigned po = wave == 5 ? (unsigned)ln * (2u << 20) + (1u << 20) : wave == 7 ? (unsigned)(32 * tt) * 1024u : (unsigned)(b * n + ln) * 128u;
        const rsrc_t rp = wave == 0 ? rWQKV : wave == 1 ? rWG : wave == 2 ? rWU : wave == 3 ? rWD : wave == 4 ? rWO : wave == 5 ? rWS : rOUT;
        asm volatile("s_mov_b32 m0, %2\n\ts_nop 0\n\tbuffer_load_dword %0, %1, 0 offen lds" ::"v"(po), "s"(rp), "s"(m0v) : "m0", "memory");
      }
    }
#define PFN 1
    RBAR();
    dma_run4(rWQKV, vq, (unsigned)(64 * wave) * 3072u + (512u + 64u * c) * 2u, 8 * 3072, LB + L_WK + (unsigned)(64 * wave) * 128u, 2);
    asm volatile("s_waitcnt vmcnt(%9) lgkmcnt(0)" : "+v"(xr[0]), "+v"(xr[1]), "+v"(xr[2]), "+v"(xr[3]), "+v"(xr[4]), "+v"(xr[5]), "+v"(xr[6]), "+v"(xr[7]), "+s"(T) : "i"(8 + PFN) : "memory");
    T += 1ull;
    STAMP(1);
    ln8(xr, LB + L_XLN + (unsigned)(wave >> 2) * 32768u + (unsigned)(lane >> 1) * 1024u + (unsigned)(lane & 1) * 512u + (unsigned)(wave & 3) * 128u, lane);
    STAMP(30);
  }
  const int Hp = H - 8 + wave;

  if (wave < 4) {
    {
      const int n = wave == 3 ? 16 : 43;
      const unsigned po = (unsigned)(b * n + (lane < n ? lane : 0)) * 128u;
      const rsrc_t rp = wave == 0 ? rWG : wave == 1 ? rWU : wave == 2 ? rWD : rWO;
      asm volatile("s_mov_b32 m0, %2\n\ts_nop 0\n\tbuffer_load_dword %0, %1, 0 offen lds" ::"v"(po), "s"(rp), "s"(L_WV + (unsigned)wave * 8192u) : "m0", "memory");
    }
    dma_run4(rWQKV, vq, (unsigned)(64 * wave) * 3072u + (1024u + 64u * c) * 2u, 8 * 3072, LB + L_WV + (unsigned)(64 * wave) * 128u, 2);
    VMWAIT(9);
    BAR();
    STAMP(2);
    const unsigned trn = LB + (unsigned)((8 * h + trq) * 128 + ((hp ^ (trq >> 1)) * 64) + trcb * 32 + trp * 8);
    unsigned trn2 = trn + 65536u, trn3 = trn + L_WQA;
    OPQ(trn2); OPQ(trn3);
    bf16x8 xf[32];
    const unsigned xb = LB + L_XLN + (unsigned)tau * 32768u + (unsigned)h * 512u;
    auto ldx = [&](int s) { xf[s] = ldsfrag(xb + (unsigned)s * 1024u + (unsigned)((r ^ ((2 * (s & 3) + h) & 7)) << 4)); };
    f32x16 aK, aV, aQ;
#pragma unroll
    for (int i = 0; i < 16; ++i) { aK[i] = 0.f; aV[i] = 0.f; aQ[i] = 0.f; }
    u32x4 kf0, kf1, vf0, vf1;
#pragma unroll
    for (int s = 0; s < 4; ++s) ldx(s);
    const unsigned kvo = (unsigned)((tt * 8 + c) * 4 + hp * 2) * 1024u + lane * 16;
    pipe<32, 4>([&](auto I_) __attribute__((always_inline)) { constexpr int i = I_.value; return trfrag(trn + i * 2048, trn + i * 2048 + 512); },
                [&](auto I_, const bf16x8& w) __attribute__((always_inline)) {
                  constexpr int i = I_.value;
                  MFMAP(aK, w, xf[i]);
                  if constexpr (i + 4 < 32) ldx(i + 4);
                },
                [&](auto) {});
    kf0 = pack8(aK, 0); kf1 = pack8(aK, 8);
    __builtin_amdgcn_raw_buffer_store_b128(kf0, rWS, OFF_K + kvo, 0, 16);
    __builtin_amdgcn_raw_buffer_store_b128(kf1, rWS, OFF_K + kvo + 1024, 0, 16);
    STAMP(3);
    VMWAIT(2);
    RBAR();
    STAMP(27);
    pipe<64, 5>(
        [&](auto I_) __attribute__((always_inline)) {
          constexpr int i = I_.value;
          const unsigned a = i < 32 ? trn2 + i * 2048 : i < 48 ? trn3 + (i - 32) * 2048 : trn + L_WQB + (i - 48) * 2048;
          return trfrag(a, a + 512);
        },
        [&](auto I_, const bf16x8& w) __attribute__((always_inline)) {
          constexpr int i = I_.value;
          if constexpr (i < 32) MFMAP(aV, xf[i], w);
          else MFMAP(aQ, w, xf[i - 32]);
          if (i == 26) {
            VMWAIT(0);
            if (lane == 0) __hip_atomic_store((GAS u64*)(ws + OFF_KF + (unsigned)(H * 8 + c) * 128u) + wave, T, RLX_AGENT);
          }
          if (i == 33) {
            vf0 = pack8(aV, 0); vf1 = pack8(aV, 8);
            __builtin_amdgcn_raw_buffer_store_b128(vf0, rWS, OFF_V + kvo, 0, 16);
            __builtin_amdgcn_raw_buffer_store_b128(vf1, rWS, OFF_V + kvo + 1024, 0, 16);
            STAMP(4);
          }
          if (i == 60) {
            VMWAIT(0);
            if (lane == 0) __hip_atomic_store((GAS u64*)(ws + OFF_VF + (unsigned)(H * 8 + c) * 128u) + wave, T, RLX_AGENT);
          }
        },
        [&](auto I_) __attribute__((always_inline)) {
          constexpr int i = I_.value;
          if (i == 16) RBAR();
          if (i == 32) RBAR();
        });
    STAMP(5);
    RBAR();
#pragma unroll
    for (int i = 0; i < 16; ++i) aQ[i] *= 0.25f * 1.44269504088896f;
    qf = pack8(aQ, 0);
    const u32x4 qf1 = pack8(aQ, 8);
    ldsst(LB + L_K + (unsigned)((8 + tau) * 4 + hp * 2) * 1024u + lane * 16, kf0);
    ldsst(LB + L_K + (unsigned)((8 + tau) * 4 + hp * 2 + 1) * 1024u + lane * 16, kf1);
    ldsst(LB + L_V + (unsigned)((8 + tau) * 4 + hp * 2) * 1024u + lane * 16, vf0);
    ldsst(LB + L_V + (unsigned)((8 + tau) * 4 + hp * 2 + 1) * 1024u + lane * 16, vf1);
    ldsst(LB + L_QH + (unsigned)wave * 1024u + lane * 16, qf1);
    { const u32x4 one4 = {0x3F803F80u, 0x3F803F80u, 0x3F803F80u, 0x3F803F80u}; ldsst(LB + L_ONES, one4); }
    dma_run4(rWO, v1k, (unsigned)(128 * wave) * 1024u + 128u * c, 8 * 1024, LB + L_WO + (unsigned)(128 * wave) * 128u, 4);
    BAR();
  } else {
    const int hw = wave - 4;
    VMWAIT(0);
    BAR();
    u32x4 st[24];
#pragma unroll
    for (int i = 0; i < 24; ++i) {
      const unsigned so = (unsigned)((i < 8 || i >= 16 ? 256 : 0) + 64 * hw + 8 * (i & 7)) * 3072u + ((i < 8 ? 1024u : 0u) + 64u * c) * 2u;
      asm volatile("buffer_load_dwordx4 %0, %1, %2, %3 offen" : "=v"(st[i]) : "v"(vq), "s"(rWQKV), "s"(so) : "memory");
    }
    STAMP(3);
    RBAR();
    asm volatile("s_waitcnt vmcnt(16)" : "+v"(st[0]), "+v"(st[1]), "+v"(st[2]), "+v"(st[3]), "+v"(st[4]), "+v"(st[5]), "+v"(st[6]), "+v"(st[7])::"memory");
#pragma unroll
    for (int i = 0; i < 8; ++i) ldsst(LB + L_WV + (unsigned)(256 + 64 * hw + 8 * i) * 128u + lane * 16, st[i]);
    BAR();
    asm volatile("s_waitcnt vmcnt(0)" : "+v"(st[8]), "+v"(st[9]), "+v"(st[10]), "+v"(st[11]), "+v"(st[12]), "+v"(st[13]), "+v"(st[14]), "+v"(st[15]),
                 "+v"(st[16]), "+v"(st[17]), "+v"(st[18]), "+v"(st[19]), "+v"(st[20]), "+v"(st[21]), "+v"(st[22]), "+v"(st[23])::"memory");
#pragma unroll
    for (int i = 0; i < 8; ++i) {
      ldsst(LB + L_WQA + (unsigned)(64 * hw + 8 * i) * 128u + lane * 16, st[8 + i]);
      ldsst(LB + L_WQB + (unsigned)(64 * hw + 8 * i) * 128u + lane * 16, st[16 + i]);
    }
    BAR();
    if (j == 0 && hw == 0) {
      if (lane < 32) __hip_atomic_store((GAS unsigned*)(ws + OFF_CNT + (unsigned)(32 * G + lane) * 128u), 0u, RLX_AGENT);
      VMWAIT(0);
      if (lane == 0) __hip_atomic_store((GAS u64*)(ws + OFF_READY + (unsigned)G * 256u), T, RLX_AGENT);
    }
    if (Hp >= 0) {
      poll_tag((const GAS u64*)(ws + OFF_KF + (unsigned)(Hp * 8 + c) * 128u) + (lane & 3), T);
      dma_run4_sc1(rWS, lane * 16, OFF_K + (unsigned)((2 * Hp) * 8 + c) * 4096u, LB + L_K + (unsigned)(8 * hw) * 1024u);
      dma_run4_sc1(rWS, lane * 16, OFF_K + (unsigned)((2 * Hp + 1) * 8 + c) * 4096u, LB + L_K + (unsigned)(8 * hw + 4) * 1024u);
    }
    STAMP(5);
    RBAR();
    VMWAIT(0);
    RBAR();
    qf = *(LAS u32x4*)(LB + L_QH + (unsigned)hw * 1024u + lane * 16);
  }
  STAMP(6);

  const int du0 = (43 * j) >> 4, ndu = ((43 * (j + 1)) >> 4) - du0;
  unsigned v3g[3], v3u[3];
#pragma unroll
  for (int i = 0; i < 3; ++i) {
    const unsigned qd = (unsigned)(i * 64 + lane), row = qd / 12u, ch = qd - row * 12u;
    v3g[i] = row * 2752u + ch * 16u;
    v3u[i] = row * 2752u + (ch ^ 2u) * 16u;
  }
  {
    const int e = wave >> 2;
    const unsigned ka = LB + L_K + (unsigned)(tau * 4 + hp * 2 + e) * 1024u + lane * 16;
    const unsigned va = LB + L_V + (unsigned)(tau * 4 + hp * 2) * 1024u + lane * 16;
    const unsigned so0 = (unsigned)(16 * wave) * 2752u + 64u * du0, ld0 = LB + (unsigned)(16 * wave) * 192u;
    auto hook = [&](int n) {
      if (n < 3) { dma16(rWG, v3g[n], so0, ld0 + n * 1024u); dma16(rWU, v3u[n], so0, ld0 + 24576u + n * 1024u); }
    };
    auto mid = [&]() {
      if (wave >= 4) {
        const int hw = wave - 4;
        poll_tag((lane == 4 || Hp < 0) ? (const GAS u64*)(ws + OFF_READY + (unsigned)G * 256u) : (const GAS u64*)(ws + OFF_VF + (unsigned)(Hp * 8 + c) * 128u) + (lane & 3), T);
        if (Hp >= 0) {
          dma_run4_sc1(rWS, lane * 16, OFF_V + (unsigned)((2 * Hp) * 8 + c) * 4096u, LB + L_V + (unsigned)(8 * hw) * 1024u);
          dma_run4_sc1(rWS, lane * 16, OFF_V + (unsigned)((2 * Hp + 1) * 8 + c) * 4096u, LB + L_V + (unsigned)(8 * hw + 4) * 1024u);
        }
      }
    };
    auto st = [&](int i) { STAMP(i); };
    u32x4 of;
    if (tt >= 8) of = attention<true>(ka, va, asfrag(qf), 0, e, r, h, wave >= 4, mid, hook, st);
    else of = attention<false>(ka, va, asfrag(qf), 8 - tt, e, r, h, wave >= 4, mid, hook, st);
    __builtin_amdgcn_raw_buffer_store_b128(of, rWS, OFF_O + (unsigned)(tt * 32 + 4 * c + 2 * hp + e) * 1024u + lane * 16, 0, 16);
  }
  STAMP(17);
  const unsigned trpm = (unsigned)((4 * h + trq) * 128 + trcb * 32 + trp * 8);

  if (wave < 4) {
    const int kh = hp;
    VMWAIT(0);
    const unsigned haveO = arrive(cntT, lane);
    RBAR();
    STAMP(8);
    u32x2 xres[4];
#pragma unroll
    for (int g = 0; g < 4; ++g)
      xres[g] = __builtin_amdgcn_raw_buffer_load_b64(rX, (unsigned)((32 * tt + r) * 1024 + (64 * c + 32 * kh + 8 * g + 4 * h) * 2), 0, 0);
    wait_cnt(cntT, haveO, 32u);
    STAMP(9);
    u32x4 of[16];
#pragma unroll
    for (int s = 0; s < 16; ++s) of[s] = __builtin_amdgcn_raw_buffer_load_b128(rWS, OFF_O + (unsigned)(tt * 32 + 16 * kh + s) * 1024u + lane * 16, 0, 16);
    const unsigned aA = LB + L_WO + (unsigned)kh * 32768u + trpm + (unsigned)((kh ^ (trq >> 1)) * 64);
    const unsigned aB = aA ^ 64u;
    f32x16 accA, accB;
#pragma unroll
    for (int i = 0; i < 16; ++i) { accA[i] = 0.f; accB[i] = 0.f; }
    pipe<32, 6>([&](auto I_) { constexpr int i = I_.value; const unsigned a = ((i & 1) ? aA : aB) + (i >> 1) * 2048; return trfrag(a, a + 1024); },
                [&](auto I_, const bf16x8& w) { constexpr int i = I_.value; if (i & 1) MFMAP(accA, w, asfrag(of[i >> 1])); else MFMAP(accB, w, asfrag(of[i >> 1])); },
                [&](auto) {});
    STAMP(18);
    RBAR();
#pragma unroll
    for (int g = 0; g < 4; ++g) { f32x4 v = {accB[4 * g], accB[4 * g + 1], accB[4 * g + 2], accB[4 * g + 3]}; *(LAS f32x4*)(LB + L_SCR + (unsigned)wave * 4096u + g * 1024 + lane * 16) = v; }
    BAR();
    unsigned hres[8];
    {
      float hv[16];
#pragma unroll
      for (int g = 0; g < 4; ++g) {
        const f32x4 v = *(LAS f32x4*)(LB + L_SCR + (unsigned)(wave ^ 1) * 4096u + g * 1024 + lane * 16);
        hres[2 * g] = pk2(accA[4 * g] + v[0] + blo(xres[g][0]), accA[4 * g + 1] + v[1] + bhi(xres[g][0]));
        hres[2 * g + 1] = pk2(accA[4 * g + 2] + v[2] + blo(xres[g][1]), accA[4 * g + 3] + v[3] + bhi(xres[g][1]));
      }
      const unsigned o = OFF_H + (unsigned)(tt * 32 + 4 * c + 2 * kh) * 1024u + lane * 16;
      u32x4 h0 = {hres[0], hres[1], hres[2], hres[3]}, h1 = {hres[4], hres[5], hres[6], hres[7]};
      __builtin_amdgcn_raw_buffer_store_b128(h0, rWS, o, 0, 16);
      __builtin_amdgcn_raw_buffer_store_b128(h1, rWS, o + 1024, 0, 16);
      float sum = 0.f;
#pragma unroll
      for (int i = 0; i < 8; ++i) { hv[2 * i] = blo(hres[i]); hv[2 * i + 1] = bhi(hres[i]); sum += hv[2 * i] + hv[2 * i + 1]; }
      const float mean = swap32_add(sum) * (1.f / 32.f);
      float m2 = 0.f;
#pragma unroll
      for (int i = 0; i < 16; ++i) { const float d = hv[i] - mean; m2 = fmaf(d, d, m2); }
      m2 = swap32_add(m2);
      u32x2 st = {__float_as_uint(mean), __float_as_uint(m2)};
      if (h == 0) __builtin_amdgcn_raw_buffer_store_b64(st, rWS, OFF_HS + (unsigned)((tt * 16 + 2 * c + kh) * 256 + r * 8), 0, 16);
    }
    STAMP(19);
    VMWAIT(0);
    RBAR();
    (void)arrive(cntT + 32, lane);
    STAMP(10);

    const int t4 = 4 * G + wave;
    GAS unsigned* cnt4 = (GAS unsigned*)(ws + OFF_CNT + (unsigned)t4 * 1024u);
    wait_cnt(cnt4 + 32, 0u, 16u);
    STAMP(11);
    const unsigned hb = OFF_H + (unsigned)t4 * 32768u + lane * 16;
    u32x4 hf[32];
#pragma unroll
    for (int s = 0; s < 12; ++s) hf[s] = __builtin_amdgcn_raw_buffer_load_b128(rWS, hb + s * 1024, 0, 16);
    u32x2 stp[8];
    const unsigned w3e = LB + (unsigned)(trcb * 24576 + (4 * h + trq) * 192 + trp * 8 + trcb * 32);
    const unsigned w3o = LB + (unsigned)(trcb * 24576 + (4 * h + trq) * 192 + trp * 8 - trcb * 32);
    unsigned w3e1 = w3e + 49152u, w3o1 = w3o + 49152u, w3e2 = w3e + 98304u, w3o2 = w3o + 98304u, csa = LB + L_SCR + (unsigned)r * 16u;
    OPQ(w3e1); OPQ(w3o1); OPQ(w3e2); OPQ(w3o2); OPQ(csa);
    f32x16 ac[6];
#pragma unroll
    for (int u = 0; u < 6; ++u)
#pragma unroll
      for (int i = 0; i < 16; ++i) ac[u][i] = 0.f;
    float rsg, irs;
    u32x4 mfr;
    float tg[8];
    const unsigned hho = OFF_HH + (unsigned)(t4 * 86 + 2 * du0) * 1024u + lane * 16;
    const unsigned hho2 = ndu == 3 ? hho + 4096u : 0x7F000000u;
    auto epi = [&](int u, int k) {
      if (k == 1) {
#pragma unroll
        for (int i = 0; i < 8; ++i) tg[i] = ac[u][i] * rsg;
      } else if (k == 2 || k == 3) {
#pragma unroll
        for (int i = 4 * (k - 2); i < 4 * (k - 1); ++i) tg[i] = __builtin_amdgcn_exp2f(tg[i]);
      } else if (k == 4) {
#pragma unroll
        for (int i = 0; i < 8; ++i) tg[i] = fmaf(tg[i], irs, irs);
      } else if (k == 5 || k == 6) {
#pragma unroll
        for (int i = 4 * (k - 5); i < 4 * (k - 4); ++i) tg[i] = __builtin_amdgcn_rcpf(tg[i]);
      } else if (k == 7) {
#pragma unroll
        for (int i = 0; i < 8; ++i) tg[i] *= ac[u][8 + i];
      } else if (k == 8) {
        u32x4 o = {pk2(tg[0], tg[1]), pk2(tg[2], tg[3]), pk2(tg[4], tg[5]), pk2(tg[6], tg[7])};
        __builtin_amdgcn_raw_buffer_store_b128(o, rWS, u < 4 ? hho + u * 1024 : hho2 + (u - 4) * 1024, 0, 16);
      }
      if (k >= 1 && k <= 7) {
#pragma unroll
        for (int i = 0; i < 8; ++i) OPQ(tg[i]);
      }
    };
    pipe<198, 6>(
        [&](auto I_) __attribute__((always_inline)) {
          constexpr int i = I_.value;
          if constexpr (i < 144) {
            constexpr int u = i % 6, s = (i % 48) / 6;
            const unsigned a = ((i / 48 == 1) ? ((u & 1) ? w3o1 : w3e1) : ((u & 1) ? w3o : w3e)) + s * 3072 + u * 32;
            return trfrag(a, a + 1536);
          } else {
            constexpr int u = (i - 144) / 9, s = (i - 144) % 9;
            if constexpr (s == 8) return ldsfrag(csa + u * 512);
            else { const unsigned a = ((u & 1) ? w3o2 : w3e2) + s * 3072 + u * 32; return trfrag(a, a + 1536); }
          }
        },
        [&](auto I_, const bf16x8& w) __attribute__((always_inline)) {
          constexpr int i = I_.value;
          if constexpr (i < 144) {
            constexpr int u = i % 6, ks = i / 6, s = ks < 16 ? ks : ks + 8;
            MFMAP(ac[u], w, asfrag(hf[s]));
            if constexpr (u == 5 && ks + 12 < 32) { constexpr int k2 = ks + 12, s2 = k2 < 16 ? k2 : k2 < 24 ? k2 + 8 : k2 - 8; hf[s2] = __builtin_amdgcn_raw_buffer_load_b128(rWS, hb + s2 * 1024, 0, 16); }
            if (i == 121) {
#pragma unroll
              for (int q = 0; q < 8; ++q) stp[q] = __builtin_amdgcn_raw_buffer_load_b64(rWS, OFF_HS + (unsigned)((t4 * 16 + 2 * q + h) * 256 + r * 8), 0, 16);
            }
          } else {
            constexpr int u = (i - 144) / 9, s = (i - 144) % 9;
            if constexpr (s == 8) MFMAP(ac[u], w, asfrag(mfr));
            else MFMAP(ac[u], w, asfrag(hf[16 + s]));
            if (i == 146) {
              float ms = 0.f, q = 0.f;
#pragma unroll
              for (int k = 0; k < 8; ++k) { ms += __uint_as_float(stp[k][0]); q += __uint_as_float(stp[k][1]); }
              const float mean = swap32_add(ms) * (1.f / 16.f);
#pragma unroll
              for (int k = 0; k < 8; ++k) { const float d = __uint_as_float(stp[k][0]) - mean; q = fmaf(32.f * d, d, q); }
              q = swap32_add(q) * (1.f / 512.f) + 1e-5f;
              const float rs = __builtin_amdgcn_rsqf(q);
              rsg = rs * -1.44269504088896f;
              irs = q * rs;
              const unsigned a1 = pk2(-mean, 0.f) & 0xFFFFu;
              const float r1 = -mean - blo(a1);
              const unsigned a2 = pk2(r1, 0.f) & 0xFFFFu;
              const unsigned a3 = pk2(r1 - blo(a2), 0.f) & 0xFFFFu;
              mfr[0] = h ? 0u : (a1 | (a2 << 16)); mfr[1] = h ? 0u : (a1 | (a3 << 16)); mfr[2] = h ? 0u : (a2 | (a1 << 16)); mfr[3] = 0u;
            }
            if constexpr (u > 0) epi(u - 1, s);
            if (i == 144 + 43) { VMWAIT(1); signal(cnt4 + 64, lane); }
          }
        },
        [&](auto I_) __attribute__((always_inline)) {
          constexpr int i = I_.value;
          if (i == 54) RBAR();
          if (i == 96) { STAMP(21); RBAR(); STAMP(22); }
          if (i == 150) { RBAR(); STAMP(23); }
        });
    STAMP(12);
#pragma unroll
    for (int k = 1; k < 9; ++k) epi(5, k);
    VMWAIT(2);
    signal(cnt4 + 96, lane);
    VMWAIT(1);
    if (ndu == 3) signal(cnt4 + 128, lane);
    RBAR();
    STAMP(13);

    const unsigned gb = OFF_HH + (unsigned)(tt * 86 + kh) * 1024u + lane * 16;
    u32x4 gf[43];
    wait_cnt(cntT + 64, 0u, 16u);
    STAMP(14);
#pragma unroll
    for (int m = 0; m < 16; ++m) gf[m] = __builtin_amdgcn_raw_buffer_load_b128(rWS, gb + Dm(m) * 2048, 0, 16);
    STAMP(24);
    RBAR();
    STAMP(25);
#pragma unroll
    for (int m = 16; m < 36; ++m) gf[m] = __builtin_amdgcn_raw_buffer_load_b128(rWS, gb + Dm(m) * 2048, 0, 16);
    const unsigned dA = LB + (unsigned)kh * 2048u + trpm + (unsigned)((kh ^ (trq >> 1)) * 64);
    const unsigned dB = dA ^ 64u;
    unsigned dA1 = dA + 49152u, dB1 = dB + 49152u, dA2 = dA + 98304u, dB2 = dB + 98304u;
    OPQ(dA1); OPQ(dB1); OPQ(dA2); OPQ(dB2);
#pragma unroll
    for (int i = 0; i < 16; ++i) { accA[i] = 0.f; accB[i] = 0.f; }
    pipe<86, 6>(
        [&](auto I_) __attribute__((always_inline)) {
          constexpr int i = I_.value;
          constexpr int m = i >> 1, p = m / 12, mm = m % 12;
          const unsigned a = ((i & 1) ? ((p == 0 || p == 3) ? dA1 : p == 1 ? dA : dA2) : ((p == 0 || p == 3) ? dB1 : p == 1 ? dB : dB2)) + mm * 4096;
          return trfrag(a, a + 1024);
        },
        [&](auto I_, const bf16x8& w) __attribute__((always_inline)) {
          constexpr int i = I_.value;
          if (i & 1) MFMAP(accA, w, asfrag(gf[i >> 1])); else MFMAP(accB, w, asfrag(gf[i >> 1]));
          if constexpr ((i & 1) && (i >> 1) < 7) gf[36 + (i >> 1)] = __builtin_amdgcn_raw_buffer_load_b128(rWS, gb + Dm(36 + (i >> 1)) * 2048, 0, 16);
        },
        [&](auto I_) __attribute__((always_inline)) {
          constexpr int i = I_.value;
          if (i == 30) RBAR();
          if (i == 48) RBAR();
          if (i == 72) RBAR();
        });
    STAMP(26);
#pragma unroll
    for (int g = 0; g < 4; ++g) { f32x4 v = {accB[4 * g], accB[4 * g + 1], accB[4 * g + 2], accB[4 * g + 3]}; *(LAS f32x4*)(LB + L_SCR + (unsigned)wave * 4096u + g * 1024 + lane * 16) = v; }
    BAR();
#pragma unroll
    for (int g = 0; g < 4; ++g) {
      const f32x4 v = *(LAS f32x4*)(LB + L_SCR + (unsigned)(wave ^ 1) * 4096u + g * 1024 + lane * 16);
      u32x2 o;
      o[0] = pk2(accA[4 * g] + v[0] + blo(hres[2 * g]), accA[4 * g + 1] + v[1] + bhi(hres[2 * g]));
      o[1] = pk2(accA[4 * g + 2] + v[2] + blo(hres[2 * g + 1]), accA[4 * g + 3] + v[3] + bhi(hres[2 * g + 1]));
      __builtin_amdgcn_raw_buffer_store_b64(o, rOUT, (unsigned)((32 * tt + r) * 1024 + (64 * c + 32 * kh + 8 * g + 4 * h) * 2), 0, 0);
    }
    STAMP(15);
  } else {
    const int hw = wave - 4;
    auto w3blk = [&](int blk, unsigned dst) {
#pragma unroll 1
      for (int p = 0; p < 2; ++p) {
        const unsigned row0 = (unsigned)(32 * hw + 16 * p), so = (128u * blk + row0) * 2752u + 64u * du0, ld = LB + dst + row0 * 192u;
#pragma unroll
        for (int i = 0; i < 3; ++i) {
          dma16(rWG, v3g[i], so, ld + i * 1024u);
          dma16(rWU, v3u[i], so, ld + 24576u + i * 1024u);
        }
      }
    };
    VMWAIT(0);
    signal(cntT, lane);
    RBAR();
    w3blk(1, 49152);
    u32x4 b3[12];
#pragma unroll
    for (int q = 0; q < 12; ++q) {
      const unsigned so = (384u + (unsigned)(32 * hw + 16 * (q / 6))) * 2752u + 64u * du0;
      if ((q % 6) < 3) asm volatile("buffer_load_dwordx4 %0, %1, %2, %3 offen" : "=v"(b3[q]) : "v"(v3g[q % 3]), "s"(rWG), "s"(so) : "memory");
      else asm volatile("buffer_load_dwordx4 %0, %1, %2, %3 offen" : "=v"(b3[q]) : "v"(v3u[q % 3]), "s"(rWU), "s"(so) : "memory");
    }
    VMWAIT(12);
    RBAR();
    RBAR();
    RBAR();
    w3blk(2, 98304);
    asm volatile("s_waitcnt vmcnt(0)" : "+v"(b3[0]), "+v"(b3[1]), "+v"(b3[2]), "+v"(b3[3]), "+v"(b3[4]), "+v"(b3[5]), "+v"(b3[6]), "+v"(b3[7]), "+v"(b3[8]), "+v"(b3[9]), "+v"(b3[10]), "+v"(b3[11])::"memory");
    if (hw == (G & 3)) {
      const int blk = G >> 2;
      if (blk == 3) {
#pragma unroll
        for (int q = 0; q < 12; ++q) ldsst(LB + L_SCR + (unsigned)(16 * (q / 6)) * 192u + ((q % 6) >= 3 ? 6144u : 0u) + (unsigned)(q % 3) * 1024u + lane * 16, b3[q]);
        asm volatile("s_waitcnt lgkmcnt(0)" ::: "memory");
      }
      const unsigned we = LB + (blk == 3 ? L_SCR + (unsigned)trcb * 6144u : (unsigned)(blk * 49152 + trcb * 24576 + 32 * hw * 192)) + (unsigned)((4 * h + trq) * 192 + trp * 8 + trcb * 32);
      const unsigned wo = we - (unsigned)(trcb * 64);
      f32x16 cs;
#pragma unroll
      for (int i = 0; i < 16; ++i) cs[i] = 0.f;
      bf16x8 f[12];
#pragma unroll
      for (int q = 0; q < 12; ++q) { const int u = q >> 1; const unsigned a = ((u & 1) ? wo : we) + (q & 1) * 3072 + u * 32; f[q] = trfrag(a, a + 1536); }
#pragma unroll
      for (int q = 0; q < 12; ++q) {
        const unsigned sel = (r == (q >> 1)) ? 0x3F803F80u : 0u;
        const u32x4 sv = {sel, sel, sel, sel};
        cs = MFMA(f[q], asfrag(sv), cs);
      }
      if (r < 6) {
        const unsigned tg32 = (unsigned)T;
#pragma unroll
        for (int g = 0; g < 16; ++g)
          __hip_atomic_store((GAS u64*)(ws + OFF_CS2 + (unsigned)((j * 16 + G) * 1536 + (r * 32 + (g >> 2) * 8 + h * 4 + (g & 3)) * 8)), ((u64)tg32 << 32) | __float_as_uint(cs[g]), RLX_AGENT);
      }
    }
    if (b == 0 && hw == 3) {
      unsigned spins = 0;
#pragma unroll 1
      for (;;) {
        bool ok = true;
#pragma unroll
        for (int i = 0; i < 4; ++i) ok &= __hip_atomic_load((const GAS u64*)(ws + OFF_KF + (unsigned)(i * 64 + lane) * 128u), RLX_AGENT) == T;
        if (__all(ok)) break;
        __builtin_amdgcn_s_sleep(2);
        if (++spins > SPIN_CAP) break;
      }
      if (lane == 0) __hip_atomic_store((GAS u64*)(ws + OFF_EPOCH), T, RLX_AGENT);
    }
    STAMP(20);
    RBAR();
#pragma unroll
    for (int q = 0; q < 12; ++q)
      ldsst(LB + (unsigned)(32 * hw + 16 * (q / 6)) * 192u + ((q % 6) >= 3 ? 24576u : 0u) + (unsigned)(q % 3) * 1024u + lane * 16, b3[q]);
    STAMP(21);
    BAR();
    if (hw < 3) {
      float pv[16];
      unsigned spins = 0;
#pragma unroll 1
      for (;;) {
        bool ok = true;
#pragma unroll
        for (int g = 0; g < 16; ++g) {
          const u64 x = __hip_atomic_load((const GAS u64*)(ws + OFF_CS2 + (unsigned)((j * 16 + g) * 1536 + (hw * 64 + lane) * 8)), RLX_AGENT);
          pv[g] = __uint_as_float((unsigned)x);
          ok &= (unsigned)(x >> 32) == (unsigned)T;
        }
        if (__all(ok)) break;
        __builtin_amdgcn_s_sleep(2);
        if (++spins > SPIN_CAP) break;
      }
      const float v = (((pv[0] + pv[1]) + (pv[2] + pv[3])) + ((pv[4] + pv[5]) + (pv[6] + pv[7]))) + (((pv[8] + pv[9]) + (pv[10] + pv[11])) + ((pv[12] + pv[13]) + (pv[14] + pv[15])));
      const unsigned c1 = pk2(v, 0.f) & 0xFFFFu;
      const float r1 = v - blo(c1);
      const unsigned c2 = pk2(r1, 0.f) & 0xFFFFu;
      const unsigned c3 = pk2(r1 - blo(c2), 0.f) & 0xFFFFu;
      const u32x4 f = {c1 | (c1 << 16), c2 | (c1 << 16), c2 | (c3 << 16), 0u};
      ldsst(LB + L_SCR + (unsigned)(hw * 64 + lane) * 16u, f);
    }
    BAR();
    auto w4 = [&](int m0, int n, unsigned dst) {
#pragma unroll 1
      for (int q = 0; q < n; ++q) {
        const int mm = hw + 4 * q;
        dma_run4(rWD, v1k, (unsigned)Dm(m0 + mm) * 32768u + 128u * c, 8192, LB + dst + (unsigned)mm * 4096u, 1);
      }
    };
    w4(0, 3, 49152);
    w4(12, 3, 0);
    RBAR();
    w4(24, 3, 98304);
    wait_cnt(cntT + 96 + 32 * hp, 0u, hp ? 11u : 16u);
    VMWAIT(0);
    RBAR();
    RBAR();
    w4(36, 2, 49152);
    VMWAIT(8);
    RBAR();
    VMWAIT(0);
    RBAR();
    RBAR();
  }
  STAMP_END();
}

extern "C" void kernel_launch(void* const* d_in, const int* in_sizes, int n_in, void* d_out, int out_size, void* d_ws, size_t ws_size,
                              hipStream_t stream) {
  (void)in_sizes; (void)n_in; (void)out_size; (void)ws_size;
  (void)hipFuncSetAttribute((const void*)fused_block, hipFuncAttributeMaxDynamicSharedMemorySize, (int)LDS_BYTES);
  hipLaunchKernelGGL(fused_block, dim3(256), dim3(512), LDS_BYTES, stream, d_in[0], d_in[1], d_in[2], d_in[4], d_in[5], d_in[6], d_out, (char*)d_ws);
}
```

```cpp
#include <hip/hip_runtime.h>
#include <hip/hip_bf16.h>
#include <utility>

typedef __bf16 bf16x8 __attribute__((ext_vector_type(8)));
typedef __bf16 bf16x4 __attribute__((ext_vector_type(4)));
typedef __bf16 bf16x2 __attribute__((ext_vector_type(2)));
typedef float f32x16 __attribute__((ext_vector_type(16)));
typedef float f32x4 __attribute__((ext_vector_type(4)));
typedef float f32x2 __attribute__((ext_vector_type(2)));
typedef unsigned u32x4 __attribute__((ext_vector_type(4)));
typedef unsigned u32x2 __attribute__((ext_vector_type(2)));
typedef unsigned long long u64;
#define LAS __attribute__((address_space(3)))
#define GAS __attribute__((address_space(1)))
#define DI __device__ __forceinline__
#define RLX_AGENT __ATOMIC_RELAXED, __HIP_MEMORY_SCOPE_AGENT
#define MFMA(a, b, c) __builtin_amdgcn_mfma_f32_32x32x16_bf16((a), (b), (c), 0, 0, 0)
#define MFMAP(acc, a, b) do { acc = MFMA(a, b, acc); asm volatile("" : "+v"(acc)); } while (0)
#define OPQ(x) asm volatile("" : "+v"(x))
typedef __amdgpu_buffer_rsrc_t rsrc_t;

constexpr unsigned OFF_EPOCH = 0;
constexpr unsigned OFF_READY = 4096;
constexpr unsigned OFF_KF = 16384;
constexpr unsigned OFF_VF = OFF_KF + 32768;
constexpr unsigned OFF_CNT = OFF_VF + 32768;
constexpr unsigned OFF_CSF = 192u << 10;
constexpr unsigned OFF_CS = 200u << 10;
constexpr unsigned OFF_CS2 = 9u * (1u << 20) + (512u << 10);
constexpr unsigned OFF_STAMP = 512u << 10;
constexpr unsigned OFF_K = 1u << 20;
constexpr unsigned OFF_V = 3u << 20;
constexpr unsigned OFF_O = 5u << 20;
constexpr unsigned OFF_H = 7u << 20;
constexpr unsigned OFF_HS = 9u << 20;
constexpr unsigned OFF_HH = 10u << 20;
constexpr unsigned WS_BYTES = 16u << 20;
constexpr unsigned KB = 1024;
constexpr unsigned L_WK = 0, L_WV = 64 * KB, L_XLN = 96 * KB, L_WQA = 128 * KB, L_WQB = 32 * KB;
constexpr unsigned L_K = 0, L_V = 48 * KB, L_QH = 40 * KB, L_ONES = 88 * KB, L_WO = 96 * KB;
constexpr unsigned L_SCR = 144 * KB;
constexpr unsigned LDS_BYTES = 160 * KB;
constexpr unsigned SPIN_CAP = 1u << 18;

#define BAR() do { asm volatile("s_waitcnt lgkmcnt(0)" ::: "memory"); __builtin_amdgcn_s_barrier(); asm volatile("" ::: "memory"); } while (0)
#define VMWAIT(n) asm volatile("s_waitcnt vmcnt(" #n ")" ::: "memory")

#define STAMP(i) do {} while (0)
#define STAMP_END() do {} while (0)

DI unsigned rfl(unsigned x) { return (unsigned)__builtin_amdgcn_readfirstlane((int)x); }
DI void dma16(rsrc_t rs, unsigned voff, unsigned soff, unsigned lds) {
  asm volatile("s_mov_b32 m0, %0\n\ts_nop 0\n\tbuffer_load_dwordx4 %1, %2, %3 offen lds" ::"s"(lds), "v"(voff), "s"(rs), "s"(soff) : "m0", "memory");
}
DI void dma16_sc1(rsrc_t rs, unsigned voff, unsigned soff, unsigned lds) {
  asm volatile("s_mov_b32 m0, %0\n\ts_nop 0\n\tbuffer_load_dwordx4 %1, %2, %3 offen sc1 lds" ::"s"(lds), "v"(voff), "s"(rs), "s"(soff) : "m0", "memory");
}
DI void dma_run4(rsrc_t rs, unsigned voff, unsigned soff, unsigned step, unsigned lds, int n4) {
#pragma unroll 1
  for (int i = 0; i < n4; ++i) {
    asm volatile(
        "s_mov_b32 m0, %[l]\n\ts_nop 0\n\tbuffer_load_dwordx4 %[v], %[r], %[s] offen lds\n\t"
        "s_add_u32 m0, m0, 0x400\n\ts_add_u32 %[s], %[s], %[st]\n\tbuffer_load_dwordx4 %[v], %[r], %[s] offen lds\n\t"
        "s_add_u32 m0, m0, 0x400\n\ts_add_u32 %[s], %[s], %[st]\n\tbuffer_load_dwordx4 %[v], %[r], %[s] offen lds\n\t"
        "s_add_u32 m0, m0, 0x400\n\ts_add_u32 %[s], %[s], %[st]\n\tbuffer_load_dwordx4 %[v], %[r], %[s] offen lds\n\t"
        "s_add_u32 %[s], %[s], %[st]\n\ts_add_u32 %[l], %[l], 0x1000"
        : [s] "+s"(soff), [l] "+s"(lds) : [v] "v"(voff), [r] "s"(rs), [st] "s"(step) : "m0", "scc", "memory");
  }
}
DI void dma_run4_sc1(rsrc_t rs, unsigned voff, unsigned soff, unsigned lds) {
  asm volatile(
      "s_mov_b32 m0, %[l]\n\ts_nop 0\n\tbuffer_load_dwordx4 %[v], %[r], %[s] offen sc1 lds\n\t"
      "buffer_load_dwordx4 %[v], %[r], %[s] offen offset:1024 sc1 lds\n\t"
      "buffer_load_dwordx4 %[v], %[r], %[s] offen offset:2048 sc1 lds\n\t"
      "buffer_load_dwordx4 %[v], %[r], %[s] offen offset:3072 sc1 lds"
      ::[s] "s"(soff), [l] "s"(lds), [v] "v"(voff), [r] "s"(rs) : "m0", "memory");
}
#define RBAR() do { asm volatile("" ::: "memory"); __builtin_amdgcn_s_barrier(); asm volatile("" ::: "memory"); } while (0)
#define SCHED() __builtin_amdgcn_sched_barrier(0)
template <int... I, class F> DI void sfor_(std::integer_sequence<int, I...>, F&& f) { (f(std::integral_constant<int, I>{}), ...); }
template <int N, class F> DI void sfor(F&& f) { sfor_(std::make_integer_sequence<int, N>{}, f); }
template <int N, int D, class LD, class MM, class PRE>
DI void pipe(LD&& ld, MM&& mm, PRE&& pre) {
  bf16x8 buf[D];
  sfor<D>([&](auto I) { pre(I); buf[I.value] = ld(I); });
  SCHED();
  sfor<N>([&](auto S) {
    constexpr int s = S.value;
    mm(S, buf[s % D]);
    if constexpr (s + D < N) { pre(std::integral_constant<int, s + D>{}); buf[s % D] = ld(std::integral_constant<int, s + D>{}); }
    SCHED();
  });
}
DI bf16x8 trfrag(unsigned a0, unsigned a1) {
  bf16x4 lo = __builtin_amdgcn_ds_read_tr16_b64_v4bf16((LAS bf16x4*)a0);
  bf16x4 hi = __builtin_amdgcn_ds_read_tr16_b64_v4bf16((LAS bf16x4*)a1);
  return __builtin_shufflevector(lo, hi, 0, 1, 2, 3, 4, 5, 6, 7);
}
DI bf16x8 ldsfrag(unsigned a) { return *(LAS bf16x8*)a; }
DI void ldsst(unsigned a, u32x4 v) { *(LAS u32x4*)a = v; }
DI unsigned pk2(float a, float b) { f32x2 v = {a, b}; return __builtin_bit_cast(unsigned, __builtin_convertvector(v, bf16x2)); }
DI u32x4 pack8(const f32x16& x, int o) { u32x4 r; r[0] = pk2(x[o], x[o + 1]); r[1] = pk2(x[o + 2], x[o + 3]); r[2] = pk2(x[o + 4], x[o + 5]); r[3] = pk2(x[o + 6], x[o + 7]); return r; }
DI float blo(unsigned u) { return __uint_as_float(u << 16); }
DI float bhi(unsigned u) { return __uint_as_float(u & 0xffff0000u); }
DI bf16x8 asfrag(u32x4 v) { return __builtin_bit_cast(bf16x8, v); }
DI float swap32_add(float a) {
  auto r = __builtin_amdgcn_permlane32_swap(__float_as_uint(a), __float_as_uint(a), false, false);
  return __uint_as_float(r[0]) + __uint_as_float(r[1]);
}
DI float swap32_max(float a) {
  auto r = __builtin_amdgcn_permlane32_swap(__float_as_uint(a), __float_as_uint(a), false, false);
  return fmaxf(__uint_as_float(r[0]), __uint_as_float(r[1]));
}
template <int CTRL> DI float dpp_add(float a) { return a + __uint_as_float(__builtin_amdgcn_update_dpp(0, __float_as_uint(a), CTRL, 0xf, 0xf, true)); }
DI float row16_sum(float a) { a = dpp_add<0x128>(a); a = dpp_add<0x124>(a); a = dpp_add<0x122>(a); a = dpp_add<0x121>(a); return a; }
DI void red8(const float (&v)[8], float& A, float& B) {
  float t[4];
#pragma unroll
  for (int i = 0; i < 4; ++i) {
    auto r = __builtin_amdgcn_permlane32_swap(__float_as_uint(v[2 * i]), __float_as_uint(v[2 * i + 1]), false, false);
    t[i] = __uint_as_float(r[0]) + __uint_as_float(r[1]);
  }
  auto r0 = __builtin_amdgcn_permlane16_swap(__float_as_uint(t[0]), __float_as_uint(t[1]), false, false);
  auto r1 = __builtin_amdgcn_permlane16_swap(__float_as_uint(t[2]), __float_as_uint(t[3]), false, false);
  A = row16_sum(__uint_as_float(r0[0]) + __uint_as_float(r0[1]));
  B = row16_sum(__uint_as_float(r1[0]) + __uint_as_float(r1[1]));
}
DI float rdl(float a, int l) { return __uint_as_float((unsigned)__builtin_amdgcn_readlane((int)__float_as_uint(a), l)); }
DI void bc8(float A, float B, float (&o)[8]) {
  o[0] = rdl(A, 0); o[2] = rdl(A, 16); o[1] = rdl(A, 32); o[3] = rdl(A, 48);
  o[4] = rdl(B, 0); o[6] = rdl(B, 16); o[5] = rdl(B, 32); o[7] = rdl(B, 48);
}
DI void wait_cnt(GAS unsigned* c, unsigned have, unsigned target) {
  unsigned spins = 0;
#pragma unroll 1
  while (have < target) {
    __builtin_amdgcn_s_sleep(4);
    have = rfl(__hip_atomic_load(c, RLX_AGENT));
    if (++spins > SPIN_CAP) break;
  }
  __builtin_amdgcn_fence(__ATOMIC_ACQUIRE, "wavefront");
}
DI unsigned arrive(GAS unsigned* c, int lane) {
  unsigned r = 0;
  if (lane == 0) r = __hip_atomic_fetch_add(c, 1u, RLX_AGENT) + 1u;
  r = rfl(r);
  asm volatile("" ::"s"(r));
  return r;
}

__host__ __device__ constexpr int Dm(int m) { return m < 32 ? ((43 * (m & 15)) >> 4) + (m >> 4) : (4 + 8 * ((m - 32) >> 1) + 3 * ((m - 32) & 1) < 42 ? 4 + 8 * ((m - 32) >> 1) + 3 * ((m - 32) & 1) : 42); }

template <bool FULL, class MID, class HOOK, class ST>
DI u32x4 attention(const unsigned ka, const unsigned va, const bf16x8 q, const int n0, const int e, const int r, const int h, const bool helper, MID&& mid, HOOK&& hook, ST&& stamp) {
  bf16x8 kf[9];
#pragma unroll
  for (int n = 0; n < 9; ++n) kf[n] = ldsfrag(ka + n * 4096);
  f32x16 zero;
#pragma unroll
  for (int i = 0; i < 16; ++i) zero[i] = 0.f;
  f32x16 sm = MFMA(kf[8], q, zero);
  f32x16 sa, sb;
  if (FULL) {
    sb = MFMA(kf[0], q, zero);
    sa = MFMA(kf[1], q, zero);
    OPQ(sa);
  } else {
#pragma unroll
    for (int i = 0; i < 16; ++i) sb[i] = -1e30f;
  }
#pragma unroll
  for (int i = 0; i < 16; ++i) { const int kl = (i & 3) + 8 * (i >> 2) + 4 * h; sm[i] = (kl <= r) ? sm[i] : sb[i]; }
  float mx = sm[0], mx2 = sm[1];
#pragma unroll
  for (int i = 2; i < 16; i += 2) mx = fmaxf(fmaxf(mx, sm[i]), sm[i + 1]);
  if (FULL) {
    SCHED();
#pragma unroll
    for (int n = 1; n < 8; ++n) {
      if (n & 1) {
        if (n < 7) { sb = MFMA(kf[n + 1], q, zero); OPQ(sb); }
        SCHED();
#pragma unroll
        for (int i = 0; i < 16; i += 4) { mx = fmaxf(fmaxf(mx, sa[i]), sa[i + 1]); mx2 = fmaxf(fmaxf(mx2, sa[i + 2]), sa[i + 3]); }
      } else {
        sa = MFMA(kf[n + 1], q, zero); OPQ(sa);
        SCHED();
#pragma unroll
        for (int i = 0; i < 16; i += 4) { mx = fmaxf(fmaxf(mx, sb[i]), sb[i + 1]); mx2 = fmaxf(fmaxf(mx2, sb[i + 2]), sb[i + 3]); }
      }
      SCHED();
      if (n == 3) mid();
    }
  } else {
    mid();
#pragma unroll
    for (int n = 1; n < 8; ++n) {
      if (n >= n0) {
        const f32x16 s = MFMA(kf[n], q, zero);
#pragma unroll
        for (int i = 0; i < 16; ++i) mx = fmaxf(mx, s[i]);
      }
    }
  }
  mx = swap32_max(fmaxf(mx, mx2));
  f32x16 negm;
#pragma unroll
  for (int i = 0; i < 16; ++i) negm[i] = -mx;
  if (FULL) { sa = MFMA(kf[1], q, negm); OPQ(sa); }
  u32x4 p0, p1, d0, d1;
  {
    f32x16 p;
#pragma unroll
    for (int i = 0; i < 16; ++i) p[i] = __builtin_amdgcn_exp2f(sm[i] - mx);
    p0 = pack8(p, 0); p1 = pack8(p, 8);
#pragma unroll
    for (int k = 0; k < 4; ++k) {
      const int i0 = 2 * k, i1 = 8 + 2 * k;
      const int ka0 = (i0 & 3) + 8 * (i0 >> 2) + 4 * h, ka1 = (i1 & 3) + 8 * (i1 >> 2) + 4 * h;
      const unsigned m0 = (ka0 <= r ? 0xFFFFu : 0u) | (ka0 + 1 <= r ? 0xFFFF0000u : 0u);
      const unsigned m1 = (ka1 <= r ? 0xFFFFu : 0u) | (ka1 + 1 <= r ? 0xFFFF0000u : 0u);
      d0[k] = p0[k] & m0; d1[k] = p1[k] & m1;
    }
    p0 ^= d0; p1 ^= d1;
  }
  OPQ(p0); OPQ(p1); OPQ(d0); OPQ(d1);
  stamp(16);
  if (helper) VMWAIT(0);
  RBAR();
  stamp(7);
  const bool ones = ((r >> 4) != e);
  unsigned vp = ones ? L_ONES : va;
  const unsigned i1k = ones ? 0u : 1024u, i3k = ones ? 0u : 3072u;
  f32x16 o = zero;
  if (FULL) {
    const unsigned v8 = vp + 32 * i1k;
    const bf16x8 w80 = ldsfrag(v8), w81 = ldsfrag(v8 + i1k);
    const bf16x8 w00 = ldsfrag(vp); vp += i1k;
    const bf16x8 w01 = ldsfrag(vp); vp += i3k;
    bf16x8 v0 = ldsfrag(vp); vp += i1k;
    bf16x8 v1 = ldsfrag(vp); vp += i3k;
    o = MFMA(w80, asfrag(d0), o);
    o = MFMA(w81, asfrag(d1), o);
    o = MFMA(w00, asfrag(p0), o);
    o = MFMA(w01, asfrag(p1), o);
    OPQ(o);
    SCHED();
#pragma unroll
    for (int n = 1; n < 8; ++n) {
      bf16x8 n0f, n1f;
      if (n < 7) {
        if (n & 1) { sb = MFMA(kf[n + 1], q, negm); OPQ(sb); } else { sa = MFMA(kf[n + 1], q, negm); OPQ(sa); }
        n0f = ldsfrag(vp); vp += i1k;
        n1f = ldsfrag(vp); vp += i3k;
        hook(n - 1);
      }
      SCHED();
      f32x16& s = (n & 1) ? sa : sb;
#pragma unroll
      for (int i = 0; i < 8; ++i) s[i] = __builtin_amdgcn_exp2f(s[i]);
      MFMAP(o, v0, asfrag(pack8(s, 0)));
      SCHED();
#pragma unroll
      for (int i = 8; i < 16; ++i) s[i] = __builtin_amdgcn_exp2f(s[i]);
      MFMAP(o, v1, asfrag(pack8(s, 8)));
      SCHED();
      v0 = n0f; v1 = n1f;
    }
  } else {
#pragma unroll
    for (int n = 0; n < 6; ++n) hook(n);
    o = MFMA(ldsfrag(vp + 32 * i1k), asfrag(d0), o);
    o = MFMA(ldsfrag(vp + 33 * i1k), asfrag(d1), o);
#pragma unroll
    for (int n = 1; n < 8; ++n) {
      if (n >= n0) {
        f32x16 p = MFMA(kf[n], q, negm);
#pragma unroll
        for (int i = 0; i < 16; ++i) p[i] = __builtin_amdgcn_exp2f(p[i]);
        o = MFMA(ldsfrag(vp + (4 * n) * i1k), asfrag(pack8(p, 0)), o);
        o = MFMA(ldsfrag(vp + (4 * n + 1) * i1k), asfrag(pack8(p, 8)), o);
      }
    }
  }
  u32x4 of;
  if (e == 0) {
    const float il = __builtin_amdgcn_rcpf(o[8]);
#pragma unroll
    for (int i = 0; i < 8; ++i) o[i] *= il;
    of = pack8(o, 0);
    OPQ(of);
  } else {
    const float il = __builtin_amdgcn_rcpf(o[0]);
#pragma unroll
    for (int i = 8; i < 16; ++i) o[i] *= il;
    of = pack8(o, 8);
    OPQ(of);
  }
  return of;
}

DI void ln8(const u32x4* xr, const unsigned wb, const int lane) {
  float s1[8], s2[8], rs[8], nm[8];
  const bf16x2 one2 = __builtin_bit_cast(bf16x2, 0x3F803F80u);
#pragma unroll
  for (int i = 0; i < 8; ++i) {
    float a = 0.f, q = 0.f;
#pragma unroll
    for (int k = 0; k < 4; ++k) {
      const unsigned xu = xr[i][k];
      const bf16x2 v = __builtin_bit_cast(bf16x2, xu);
      a = __builtin_amdgcn_fdot2_f32_bf16(v, one2, a, false);
      q = __builtin_amdgcn_fdot2_f32_bf16(v, v, q, false);
    }
    s1[i] = a; s2[i] = q;
  }
  float A1, B1, A2, B2;
  red8(s1, A1, B1);
  red8(s2, A2, B2);
  A1 *= (1.f / 512.f); B1 *= (1.f / 512.f);
  A2 = __builtin_amdgcn_rsqf(fmaxf(fmaf(-A1, A1, A2 * (1.f / 512.f)), 0.f) + 1e-5f);
  B2 = __builtin_amdgcn_rsqf(fmaxf(fmaf(-B1, B1, B2 * (1.f / 512.f)), 0.f) + 1e-5f);
  bc8(A2, B2, rs);
  bc8(-A1 * A2, -B1 * B2, nm);
#pragma unroll
  for (int i = 0; i < 8; ++i) {
    u32x4 o;
#pragma unroll
    for (int k = 0; k < 4; ++k) o[k] = pk2(fmaf(blo(xr[i][k]), rs[i], nm[i]), fmaf(bhi(xr[i][k]), rs[i], nm[i]));
    ldsst(wb + (unsigned)((i ^ (lane & 7)) << 4), o);
  }
}
DI void poll_tag(const GAS u64* p, const u64 T) {
  unsigned spins = 0;
#pragma unroll 1
  for (;;) {
    const u64 v = __hip_atomic_load(p, RLX_AGENT);
    if (__all(v == T)) break;
    __builtin_amdgcn_s_sleep(1);
    if (++spins > SPIN_CAP) break;
  }
  asm volatile("" ::: "memory");
}
DI void signal(GAS unsigned* c, int lane) { if (lane == 0) (void)__hip_atomic_fetch_add(c, 1u, RLX_AGENT); }

__global__ void __launch_bounds__(512, 2)
fused_block(const void* __restrict__ gx, const void* __restrict__ gwqkv, const void* __restrict__ gwo, const void* __restrict__ gwg,
            const void* __restrict__ gwu, const void* __restrict__ gwd, void* __restrict__ gout, char* ws) {
  extern __shared__ __attribute__((aligned(16))) char smem[];
  const int lane = threadIdx.x & 63;
  const int wave = (int)rfl(threadIdx.x >> 6);
  STAMP(0);
  const int b = blockIdx.x, xcd = b & 7, idx = b >> 3;
  const int G = xcd * 2 + (idx >> 4), j = idx & 15, t = j >> 3, c = j & 7, H = 2 * G + t;
  const int r = lane & 31, h = lane >> 5;
  constexpr unsigned LB = 0;
  asm volatile("" ::"v"(smem));
  const rsrc_t rX = __builtin_amdgcn_make_buffer_rsrc((void*)gx, 0, 2048 * 512 * 2, 0x00020000);
  const rsrc_t rWQKV = __builtin_amdgcn_make_buffer_rsrc((void*)gwqkv, 0, 512 * 1536 * 2, 0x00020000);
  const rsrc_t rWO = __builtin_amdgcn_make_buffer_rsrc((void*)gwo, 0, 512 * 512 * 2, 0x00020000);
  const rsrc_t rWG = __builtin_amdgcn_make_buffer_rsrc((void*)gwg, 0, 512 * 1376 * 2, 0x00020000);
  const rsrc_t rWU = __builtin_amdgcn_make_buffer_rsrc((void*)gwu, 0, 512 * 1376 * 2, 0x00020000);
  const rsrc_t rWD = __builtin_amdgcn_make_buffer_rsrc((void*)gwd, 0, 1376 * 512 * 2, 0x00020000);
  const rsrc_t rOUT = __builtin_amdgcn_make_buffer_rsrc(gout, 0, 2048 * 512 * 2, 0x00020000);
  const rsrc_t rWS = __builtin_amdgcn_make_buffer_rsrc((void*)ws, 0, WS_BYTES, 0x00020000);

  const unsigned chsw = (unsigned)(((lane & 7) ^ (((lane >> 4) & 1) << 2)) * 16);
  const unsigned v1k = (unsigned)(lane >> 3) * 1024u + chsw;
  const int trq = (lane & 15) >> 2, trp = lane & 3, trcb = (lane >> 4) & 1;
  const int tau = (wave & 3) >> 1, hp = wave & 1;
  const int tt = 2 * H + tau;
  GAS unsigned* cntT = (GAS unsigned*)(ws + OFF_CNT + (unsigned)tt * 1024u);
  u32x4 qf;
  u32x2 xres[4];
  u64 T;

  const unsigned vq = (unsigned)(lane >> 3) * 3072u + chsw;
  {
    asm volatile("s_load_dwordx2 %0, %1, 0x0" : "=s"(T) : "s"(ws));
    if (j == 0 && wave == 4 && lane < 32) __hip_atomic_store((GAS unsigned*)(ws + OFF_CNT + (unsigned)(32 * G + lane) * 128u), 0u, RLX_AGENT);
    u32x4 xr[8];
    const unsigned vo = lane * 16, so = (unsigned)(64 * H + 8 * wave) * 1024u;
#pragma unroll
    for (int i = 0; i < 8; ++i)
      asm volatile("buffer_load_dwordx4 %0, %1, %2, %3 offen offset:%4" : "=v"(xr[i]) : "v"(vo + (i >> 2) * 4096), "s"(rX), "s"(so), "i"((i & 3) * 1024) : "memory");
    {
      const unsigned m0v = L_WK + (unsigned)wave * 8192u;
      if (wave == 6) {
        u64 pc;
        asm volatile("s_getpc_b64 %0" : "=s"(pc));
        const u64 a = (pc & ~127ull) + (u64)((idx * 8 + (lane & 7)) * 128);
        asm volatile("s_mov_b32 m0, %1\n\ts_nop 0\n\tglobal_load_lds_dword %0, off" ::"v"(a), "s"(m0v) : "m0", "memory");
      } else {
        const int n = wave == 0 ? 48 : wave == 5 ? 8 : 1;
        const int ln = lane < n ? lane : 0;
        const unsigned po = wave == 5 ? (unsigned)ln * (2u << 20) + (1u << 20) : wave == 7 ? (unsigned)(32 * tt) * 1024u : (unsigned)(b * n + ln) * 128u;
        const rsrc_t rp = wave == 0 ? rWQKV : wave == 1 ? rWG : wave == 2 ? rWU : wave == 3 ? rWD : wave == 4 ? rWO : wave == 5 ? rWS : rOUT;
        asm volatile("s_mov_b32 m0, %2\n\ts_nop 0\n\tbuffer_load_dword %0, %1, 0 offen lds" ::"v"(po), "s"(rp), "s"(m0v) : "m0", "memory");
      }
    }
#define PFN 1
    RBAR();
    dma_run4(rWQKV, vq, (unsigned)(64 * wave) * 3072u + (512u + 64u * c) * 2u, 8 * 3072, LB + L_WK + (unsigned)(64 * wave) * 128u, 2);
    asm volatile("s_waitcnt vmcnt(%9) lgkmcnt(0)" : "+v"(xr[0]), "+v"(xr[1]), "+v"(xr[2]), "+v"(xr[3]), "+v"(xr[4]), "+v"(xr[5]), "+v"(xr[6]), "+v"(xr[7]), "+s"(T) : "i"(8 + PFN) : "memory");
    T += 1ull;
    STAMP(1);
    ln8(xr, LB + L_XLN + (unsigned)(wave >> 2) * 32768u + (unsigned)(lane >> 1) * 1024u + (unsigned)(lane & 1) * 512u + (unsigned)(wave & 3) * 128u, lane);
    STAMP(30);
  }
  const int Hp = H - 8 + wave;

  if (wave < 4) {
    {
      const int n = wave == 3 ? 16 : 43;
      const unsigned po = (unsigned)(b * n + (lane < n ? lane : 0)) * 128u;
      const rsrc_t rp = wave == 0 ? rWG : wave == 1 ? rWU : wave == 2 ? rWD : rWO;
      asm volatile("s_mov_b32 m0, %2\n\ts_nop 0\n\tbuffer_load_dword %0, %1, 0 offen lds" ::"v"(po), "s"(rp), "s"(L_WV + (unsigned)wave * 8192u) : "m0", "memory");
    }
    dma_run4(rWQKV, vq, (unsigned)(64 * wave) * 3072u + (1024u + 64u * c) * 2u, 8 * 3072, LB + L_WV + (unsigned)(64 * wave) * 128u, 2);
    VMWAIT(9);
    BAR();
    STAMP(2);
    const unsigned trn = LB + (unsigned)((8 * h + trq) * 128 + ((hp ^ (trq >> 1)) * 64) + trcb * 32 + trp * 8);
    unsigned trn2 = trn + 65536u, trn3 = trn + L_WQA;
    OPQ(trn2); OPQ(trn3);
    bf16x8 xf[32];
    const unsigned xb = LB + L_XLN + (unsigned)tau * 32768u + (unsigned)h * 512u;
    auto ldx = [&](int s) { xf[s] = ldsfrag(xb + (unsigned)s * 1024u + (unsigned)((r ^ ((2 * (s & 3) + h) & 7)) << 4)); };
    f32x16 aK, aV, aQ;
#pragma unroll
    for (int i = 0; i < 16; ++i) { aK[i] = 0.f; aV[i] = 0.f; aQ[i] = 0.f; }
    u32x4 kf0, kf1, vf0, vf1;
#pragma unroll
    for (int s = 0; s < 4; ++s) ldx(s);
    const unsigned kvo = (unsigned)((tt * 8 + c) * 4 + hp * 2) * 1024u + lane * 16;
    pipe<32, 4>([&](auto I_) __attribute__((always_inline)) { constexpr int i = I_.value; return trfrag(trn + i * 2048, trn + i * 2048 + 512); },
                [&](auto I_, const bf16x8& w) __attribute__((always_inline)) {
                  constexpr int i = I_.value;
                  MFMAP(aK, w, xf[i]);
                  if constexpr (i + 4 < 32) ldx(i + 4);
                },
                [&](auto) {});
    kf0 = pack8(aK, 0); kf1 = pack8(aK, 8);
    __builtin_amdgcn_raw_buffer_store_b128(kf0, rWS, OFF_K + kvo, 0, 16);
    __builtin_amdgcn_raw_buffer_store_b128(kf1, rWS, OFF_K + kvo + 1024, 0, 16);
    STAMP(3);
    VMWAIT(2);
    RBAR();
    STAMP(27);
    pipe<64, 5>(
        [&](auto I_) __attribute__((always_inline)) {
          constexpr int i = I_.value;
          const unsigned a = i < 32 ? trn2 + i * 2048 : i < 48 ? trn3 + (i - 32) * 2048 : trn + L_WQB + (i - 48) * 2048;
          return trfrag(a, a + 512);
        },
        [&](auto I_, const bf16x8& w) __attribute__((always_inline)) {
          constexpr int i = I_.value;
          if constexpr (i < 32) MFMAP(aV, xf[i], w);
          else MFMAP(aQ, w, xf[i - 32]);
          if (i == 26) {
            VMWAIT(0);
            if (lane == 0) __hip_atomic_store((GAS u64*)(ws + OFF_KF + (unsigned)(H * 8 + c) * 128u) + wave, T, RLX_AGENT);
          }
          if (i == 33) {
            vf0 = pack8(aV, 0); vf1 = pack8(aV, 8);
            __builtin_amdgcn_raw_buffer_store_b128(vf0, rWS, OFF_V + kvo, 0, 16);
            __builtin_amdgcn_raw_buffer_store_b128(vf1, rWS, OFF_V + kvo + 1024, 0, 16);
            STAMP(4);
          }
          if (i == 60) {
            VMWAIT(0);
            if (lane == 0) __hip_atomic_store((GAS u64*)(ws + OFF_VF + (unsigned)(H * 8 + c) * 128u) + wave, T, RLX_AGENT);
          }
        },
        [&](auto I_) __attribute__((always_inline)) {
          constexpr int i = I_.value;
          if (i == 16) RBAR();
          if (i == 32) RBAR();
        });
    STAMP(5);
    RBAR();
#pragma unroll
    for (int i = 0; i < 16; ++i) aQ[i] *= 0.25f * 1.44269504088896f;
    qf = pack8(aQ, 0);
    const u32x4 qf1 = pack8(aQ, 8);
    ldsst(LB + L_K + (unsigned)((8 + tau) * 4 + hp * 2) * 1024u + lane * 16, kf0);
    ldsst(LB + L_K + (unsigned)((8 + tau) * 4 + hp * 2 + 1) * 1024u + lane * 16, kf1);
    ldsst(LB + L_V + (unsigned)((8 + tau) * 4 + hp * 2) * 1024u + lane * 16, vf0);
    ldsst(LB + L_V + (unsigned)((8 + tau) * 4 + hp * 2 + 1) * 1024u + lane * 16, vf1);
    ldsst(LB + L_QH + (unsigned)wave * 1024u + lane * 16, qf1);
    { const u32x4 one4 = {0x3F803F80u, 0x3F803F80u, 0x3F803F80u, 0x3F803F80u}; ldsst(LB + L_ONES, one4); }
#pragma unroll
    for (int g = 0; g < 4; ++g)
      xres[g] = __builtin_amdgcn_raw_buffer_load_b64(rX, (unsigned)((32 * tt + r) * 1024 + (64 * c + 32 * hp + 8 * g + 4 * h) * 2), 0, 0);
    BAR();
  } else {
    const int hw = wave - 4;
    VMWAIT(0);
    BAR();
    if (j == 0 && hw == 0 && lane == 0) __hip_atomic_store((GAS u64*)(ws + OFF_READY + (unsigned)G * 256u), T, RLX_AGENT);
    u32x4 st[24];
#pragma unroll
    for (int i = 0; i < 24; ++i) {
      const unsigned so = (unsigned)((i < 8 || i >= 16 ? 256 : 0) + 64 * hw + 8 * (i & 7)) * 3072u + ((i < 8 ? 1024u : 0u) + 64u * c) * 2u;
      asm volatile("buffer_load_dwordx4 %0, %1, %2, %3 offen" : "=v"(st[i]) : "v"(vq), "s"(rWQKV), "s"(so) : "memory");
    }
    STAMP(3);
    RBAR();
    asm volatile("s_waitcnt vmcnt(16)" : "+v"(st[0]), "+v"(st[1]), "+v"(st[2]), "+v"(st[3]), "+v"(st[4]), "+v"(st[5]), "+v"(st[6]), "+v"(st[7])::"memory");
#pragma unroll
    for (int i = 0; i < 8; ++i) ldsst(LB + L_WV + (unsigned)(256 + 64 * hw + 8 * i) * 128u + lane * 16, st[i]);
    BAR();
    asm volatile("s_waitcnt vmcnt(0)" : "+v"(st[8]), "+v"(st[9]), "+v"(st[10]), "+v"(st[11]), "+v"(st[12]), "+v"(st[13]), "+v"(st[14]), "+v"(st[15]),
                 "+v"(st[16]), "+v"(st[17]), "+v"(st[18]), "+v"(st[19]), "+v"(st[20]), "+v"(st[21]), "+v"(st[22]), "+v"(st[23])::"memory");
#pragma unroll
    for (int i = 0; i < 8; ++i) {
      ldsst(LB + L_WQA + (unsigned)(64 * hw + 8 * i) * 128u + lane * 16, st[8 + i]);
      ldsst(LB + L_WQB + (unsigned)(64 * hw + 8 * i) * 128u + lane * 16, st[16 + i]);
    }
    BAR();
    if (Hp >= 0) {
      poll_tag((const GAS u64*)(ws + OFF_KF + (unsigned)(Hp * 8 + c) * 128u) + (lane & 3), T);
      dma_run4_sc1(rWS, lane * 16, OFF_K + (unsigned)((2 * Hp) * 8 + c) * 4096u, LB + L_K + (unsigned)(8 * hw) * 1024u);
      dma_run4_sc1(rWS, lane * 16, OFF_K + (unsigned)((2 * Hp + 1) * 8 + c) * 4096u, LB + L_K + (unsigned)(8 * hw + 4) * 1024u);
    }
    STAMP(5);
    RBAR();
    dma_run4(rWO, v1k, (unsigned)(128 * hw) * 1024u + 128u * c, 8 * 1024, LB + L_WO + (unsigned)(128 * hw) * 128u, 4);
    VMWAIT(16);
    RBAR();
    qf = *(LAS u32x4*)(LB + L_QH + (unsigned)hw * 1024u + lane * 16);
#pragma unroll
    for (int g = 0; g < 4; ++g) { xres[g][0] = 0u; xres[g][1] = 0u; }
  }
  STAMP(6);

  const int du0 = (43 * j) >> 4, ndu = ((43 * (j + 1)) >> 4) - du0;
  unsigned v3g[3], v3u[3];
#pragma unroll
  for (int i = 0; i < 3; ++i) {
    const unsigned qd = (unsigned)(i * 64 + lane), row = qd / 12u, ch = qd - row * 12u;
    v3g[i] = row * 2752u + ch * 16u;
    v3u[i] = row * 2752u + (ch ^ 2u) * 16u;
  }
  {
    const int e = wave >> 2;
    const unsigned ka = LB + L_K + (unsigned)(tau * 4 + hp * 2 + e) * 1024u + lane * 16;
    const unsigned va = LB + L_V + (unsigned)(tau * 4 + hp * 2) * 1024u + lane * 16;
    const unsigned so0 = (unsigned)(16 * wave) * 2752u + 64u * du0, ld0 = LB + (unsigned)(16 * wave) * 192u;
    auto hook = [&](int n) {
      if (n < 3) { dma16(rWG, v3g[n], so0, ld0 + n * 1024u); dma16(rWU, v3u[n], so0, ld0 + 24576u + n * 1024u); }
    };
    auto mid = [&]() {
      if (wave >= 4) {
        const int hw = wave - 4;
        poll_tag((lane == 4 || Hp < 0) ? (const GAS u64*)(ws + OFF_READY + (unsigned)G * 256u) : (const GAS u64*)(ws + OFF_VF + (unsigned)(Hp * 8 + c) * 128u) + (lane & 3), T);
        if (Hp >= 0) {
          dma_run4_sc1(rWS, lane * 16, OFF_V + (unsigned)((2 * Hp) * 8 + c) * 4096u, LB + L_V + (unsigned)(8 * hw) * 1024u);
          dma_run4_sc1(rWS, lane * 16, OFF_V + (unsigned)((2 * Hp + 1) * 8 + c) * 4096u, LB + L_V + (unsigned)(8 * hw + 4) * 1024u);
        }
      }
    };
    auto st = [&](int i) { STAMP(i); };
    u32x4 of;
    if (tt >= 8) of = attention<true>(ka, va, asfrag(qf), 0, e, r, h, wave >= 4, mid, hook, st);
    else of = attention<false>(ka, va, asfrag(qf), 8 - tt, e, r, h, wave >= 4, mid, hook, st);
    __builtin_amdgcn_raw_buffer_store_b128(of, rWS, OFF_O + (unsigned)(tt * 32 + 4 * c + 2 * hp + e) * 1024u + lane * 16, 0, 16);
  }
  STAMP(17);
  const unsigned trpm = (unsigned)((4 * h + trq) * 128 + trcb * 32 + trp * 8);

  if (wave < 4) {
    const int kh = hp;
    VMWAIT(0);
    const unsigned haveO = arrive(cntT, lane);
    RBAR();
    STAMP(8);
    wait_cnt(cntT, haveO, 32u);
    STAMP(9);
    u32x4 of[16];
#pragma unroll
    for (int s = 0; s < 16; ++s) of[s] = __builtin_amdgcn_raw_buffer_load_b128(rWS, OFF_O + (unsigned)(tt * 32 + 16 * kh + s) * 1024u + lane * 16, 0, 16);
    const unsigned aA = LB + L_WO + (unsigned)kh * 32768u + trpm + (unsigned)((kh ^ (trq >> 1)) * 64);
    const unsigned aB = aA ^ 64u;
    f32x16 accA, accB;
#pragma unroll
    for (int i = 0; i < 16; ++i) { accA[i] = 0.f; accB[i] = 0.f; }
    pipe<32, 6>([&](auto I_) { constexpr int i = I_.value; const unsigned a = ((i & 1) ? aA : aB) + (i >> 1) * 2048; return trfrag(a, a + 1024); },
                [&](auto I_, const bf16x8& w) { constexpr int i = I_.value; if (i & 1) MFMAP(accA, w, asfrag(of[i >> 1])); else MFMAP(accB, w, asfrag(of[i >> 1])); },
                [&](auto) {});
    STAMP(18);
    RBAR();
#pragma unroll
    for (int g = 0; g < 4; ++g) { f32x4 v = {accB[4 * g], accB[4 * g + 1], accB[4 * g + 2], accB[4 * g + 3]}; *(LAS f32x4*)(LB + L_SCR + (unsigned)wave * 4096u + g * 1024 + lane * 16) = v; }
    BAR();
    unsigned hres[8];
    {
      float hv[16];
#pragma unroll
      for (int g = 0; g < 4; ++g) {
        const f32x4 v = *(LAS f32x4*)(LB + L_SCR + (unsigned)(wave ^ 1) * 4096u + g * 1024 + lane * 16);
        hres[2 * g] = pk2(accA[4 * g] + v[0] + blo(xres[g][0]), accA[4 * g + 1] + v[1] + bhi(xres[g][0]));
        hres[2 * g + 1] = pk2(accA[4 * g + 2] + v[2] + blo(xres[g][1]), accA[4 * g + 3] + v[3] + bhi(xres[g][1]));
      }
      const unsigned o = OFF_H + (unsigned)(tt * 32 + 4 * c + 2 * kh) * 1024u + lane * 16;
      u32x4 h0 = {hres[0], hres[1], hres[2], hres[3]}, h1 = {hres[4], hres[5], hres[6], hres[7]};
      __builtin_amdgcn_raw_buffer_store_b128(h0, rWS, o, 0, 16);
      __builtin_amdgcn_raw_buffer_store_b128(h1, rWS, o + 1024, 0, 16);
      float sum = 0.f;
#pragma unroll
      for (int i = 0; i < 8; ++i) { hv[2 * i] = blo(hres[i]); hv[2 * i + 1] = bhi(hres[i]); sum += hv[2 * i] + hv[2 * i + 1]; }
      const float mean = swap32_add(sum) * (1.f / 32.f);
      float m2 = 0.f;
#pragma unroll
      for (int i = 0; i < 16; ++i) { const float d = hv[i] - mean; m2 = fmaf(d, d, m2); }
      m2 = swap32_add(m2);
      u32x2 st = {__float_as_uint(mean), __float_as_uint(m2)};
      if (h == 0) __builtin_amdgcn_raw_buffer_store_b64(st, rWS, OFF_HS + (unsigned)((tt * 16 + 2 * c + kh) * 256 + r * 8), 0, 16);
    }
    STAMP(19);
    VMWAIT(0);
    RBAR();
    (void)arrive(cntT + 32, lane);
    STAMP(10);

    const int t4 = 4 * G + wave;
    GAS unsigned* cnt4 = (GAS unsigned*)(ws + OFF_CNT + (unsigned)t4 * 1024u);
    wait_cnt(cnt4 + 32, 0u, 16u);
    STAMP(11);
    const unsigned hb = OFF_H + (unsigned)t4 * 32768u + lane * 16;
    u32x4 hf[32];
#pragma unroll
    for (int s = 0; s < 12; ++s) hf[s] = __builtin_amdgcn_raw_buffer_load_b128(rWS, hb + s * 1024, 0, 16);
    u32x2 stp[8];
    const unsigned w3e = LB + (unsigned)(trcb * 24576 + (4 * h + trq) * 192 + trp * 8 + trcb * 32);
    const unsigned w3o = LB + (unsigned)(trcb * 24576 + (4 * h + trq) * 192 + trp * 8 - trcb * 32);
    unsigned w3e1 = w3e + 49152u, w3o1 = w3o + 49152u, w3e2 = w3e + 98304u, w3o2 = w3o + 98304u, csa = LB + L_SCR + (unsigned)r * 16u;
    OPQ(w3e1); OPQ(w3o1); OPQ(w3e2); OPQ(w3o2); OPQ(csa);
    f32x16 ac[6];
#pragma unroll
    for (int u = 0; u < 6; ++u)
#pragma unroll
      for (int i = 0; i < 16; ++i) ac[u][i] = 0.f;
    float rsg, irs;
    u32x4 mfr;
    float tg[8];
    const unsigned hho = OFF_HH + (unsigned)(t4 * 86 + 2 * du0) * 1024u + lane * 16;
    const unsigned hho2 = ndu == 3 ? hho + 4096u : 0x7F000000u;
    auto epi = [&](int u, int k) {
      if (k == 1) {
#pragma unroll
        for (int i = 0; i < 8; ++i) tg[i] = ac[u][i] * rsg;
      } else if (k == 2 || k == 3) {
#pragma unroll
        for (int i = 4 * (k - 2); i < 4 * (k - 1); ++i) tg[i] = __builtin_amdgcn_exp2f(tg[i]);
      } else if (k == 4) {
#pragma unroll
        for (int i = 0; i < 8; ++i) tg[i] = fmaf(tg[i], irs, irs);
      } else if (k == 5 || k == 6) {
#pragma unroll
        for (int i = 4 * (k - 5); i < 4 * (k - 4); ++i) tg[i] = __builtin_amdgcn_rcpf(tg[i]);
      } else if (k == 7) {
#pragma unroll
        for (int i = 0; i < 8; ++i) tg[i] *= ac[u][8 + i];
      } else if (k == 8) {
        u32x4 o = {pk2(tg[0], tg[1]), pk2(tg[2], tg[3]), pk2(tg[4], tg[5]), pk2(tg[6], tg[7])};
        __builtin_amdgcn_raw_buffer_store_b128(o, rWS, u < 4 ? hho + u * 1024 : hho2 + (u - 4) * 1024, 0, 16);
      }
      if (k >= 1 && k <= 7) {
#pragma unroll
        for (int i = 0; i < 8; ++i) OPQ(tg[i]);
      }
    };
    pipe<198, 6>(
        [&](auto I_) __attribute__((always_inline)) {
          constexpr int i = I_.value;
          if constexpr (i < 144) {
            constexpr int u = i % 6, s = (i % 48) / 6;
            const unsigned a = ((i / 48 == 1) ? ((u & 1) ? w3o1 : w3e1) : ((u & 1) ? w3o : w3e)) + s * 3072 + u * 32;
            return trfrag(a, a + 1536);
          } else {
            constexpr int u = (i - 144) / 9, s = (i - 144) % 9;
            if constexpr (s == 8) return ldsfrag(csa + u * 512);
            else { const unsigned a = ((u & 1) ? w3o2 : w3e2) + s * 3072 + u * 32; return trfrag(a, a + 1536); }
          }
        },
        [&](auto I_, const bf16x8& w) __attribute__((always_inline)) {
          constexpr int i = I_.value;
          if constexpr (i < 144) {
            constexpr int u = i % 6, ks = i / 6, s = ks < 16 ? ks : ks + 8;
            MFMAP(ac[u], w, asfrag(hf[s]));
            if constexpr (u == 5 && ks + 12 < 32) { constexpr int k2 = ks + 12, s2 = k2 < 16 ? k2 : k2 < 24 ? k2 + 8 : k2 - 8; hf[s2] = __builtin_amdgcn_raw_buffer_load_b128(rWS, hb + s2 * 1024, 0, 16); }
            if (i == 121) {
#pragma unroll
              for (int q = 0; q < 8; ++q) stp[q] = __builtin_amdgcn_raw_buffer_load_b64(rWS, OFF_HS + (unsigned)((t4 * 16 + 2 * q + h) * 256 + r * 8), 0, 16);
            }
          } else {
            constexpr int u = (i - 144) / 9, s = (i - 144) % 9;
            if constexpr (s == 8) MFMAP(ac[u], w, asfrag(mfr));
            else MFMAP(ac[u], w, asfrag(hf[16 + s]));
            if (i == 146) {
              float ms = 0.f, q = 0.f;
#pragma unroll
              for (int k = 0; k < 8; ++k) { ms += __uint_as_float(stp[k][0]); q += __uint_as_float(stp[k][1]); }
              const float mean = swap32_add(ms) * (1.f / 16.f);
#pragma unroll
              for (int k = 0; k < 8; ++k) { const float d = __uint_as_float(stp[k][0]) - mean; q = fmaf(32.f * d, d, q); }
              q = swap32_add(q) * (1.f / 512.f) + 1e-5f;
              const float rs = __builtin_amdgcn_rsqf(q);
              rsg = rs * -1.44269504088896f;
              irs = q * rs;
              const unsigned a1 = pk2(-mean, 0.f) & 0xFFFFu;
              const float r1 = -mean - blo(a1);
              const unsigned a2 = pk2(r1, 0.f) & 0xFFFFu;
              const unsigned a3 = pk2(r1 - blo(a2), 0.f) & 0xFFFFu;
              mfr[0] = h ? 0u : (a1 | (a2 << 16)); mfr[1] = h ? 0u : (a1 | (a3 << 16)); mfr[2] = h ? 0u : (a2 | (a1 << 16)); mfr[3] = 0u;
            }
            if constexpr (u > 0) epi(u - 1, s);
            if (i == 144 + 43) { VMWAIT(1); signal(cnt4 + 64, lane); }
          }
        },
        [&](auto I_) __attribute__((always_inline)) {
          constexpr int i = I_.value;
          if (i == 54) RBAR();
          if (i == 96) { STAMP(21); RBAR(); STAMP(22); }
          if (i == 150) { RBAR(); STAMP(23); }
        });
    STAMP(12);
#pragma unroll
    for (int k = 1; k < 9; ++k) epi(5, k);
    VMWAIT(2);
    signal(cnt4 + 96, lane);
    VMWAIT(1);
    if (ndu == 3) signal(cnt4 + 128, lane);
    RBAR();
    STAMP(13);

    const unsigned gb = OFF_HH + (unsigned)(tt * 86 + kh) * 1024u + lane * 16;
    u32x4 gf[43];
    wait_cnt(cntT + 64, 0u, 16u);
    STAMP(14);
#pragma unroll
    for (int m = 0; m < 16; ++m) gf[m] = __builtin_amdgcn_raw_buffer_load_b128(rWS, gb + Dm(m) * 2048, 0, 16);
    STAMP(24);
    RBAR();
    STAMP(25);
#pragma unroll
    for (int m = 16; m < 36; ++m) gf[m] = __builtin_amdgcn_raw_buffer_load_b128(rWS, gb + Dm(m) * 2048, 0, 16);
    const unsigned dA = LB + (unsigned)kh * 2048u + trpm + (unsigned)((kh ^ (trq >> 1)) * 64);
    const unsigned dB = dA ^ 64u;
    unsigned dA1 = dA + 49152u, dB1 = dB + 49152u, dA2 = dA + 98304u, dB2 = dB + 98304u;
    OPQ(dA1); OPQ(dB1); OPQ(dA2); OPQ(dB2);
#pragma unroll
    for (int i = 0; i < 16; ++i) { accA[i] = 0.f; accB[i] = 0.f; }
    pipe<86, 6>(
        [&](auto I_) __attribute__((always_inline)) {
          constexpr int i = I_.value;
          constexpr int m = i >> 1, p = m / 12, mm = m % 12;
          const unsigned a = ((i & 1) ? ((p == 0 || p == 3) ? dA1 : p == 1 ? dA : dA2) : ((p == 0 || p == 3) ? dB1 : p == 1 ? dB : dB2)) + mm * 4096;
          return trfrag(a, a + 1024);
        },
        [&](auto I_, const bf16x8& w) __attribute__((always_inline)) {
          constexpr int i = I_.value;
          if (i & 1) MFMAP(accA, w, asfrag(gf[i >> 1])); else MFMAP(accB, w, asfrag(gf[i >> 1]));
          if constexpr ((i & 1) && (i >> 1) < 7) gf[36 + (i >> 1)] = __builtin_amdgcn_raw_buffer_load_b128(rWS, gb + Dm(36 + (i >> 1)) * 2048, 0, 16);
        },
        [&](auto I_) __attribute__((always_inline)) {
          constexpr int i = I_.value;
          if (i == 30) RBAR();
          if (i == 48) RBAR();
          if (i == 72) RBAR();
        });
    STAMP(26);
    RBAR();
    {
      const unsigned ob = LB + (unsigned)kh * 17408u + (unsigned)(32 * tau + r) * 272u + (unsigned)h * 16u;
#pragma unroll
      for (int g = 0; g < 4; ++g) {
        const f32x4 va = {accA[4 * g] + blo(hres[2 * g]), accA[4 * g + 1] + bhi(hres[2 * g]), accA[4 * g + 2] + blo(hres[2 * g + 1]), accA[4 * g + 3] + bhi(hres[2 * g + 1])};
        const f32x4 vb = {accB[4 * g], accB[4 * g + 1], accB[4 * g + 2], accB[4 * g + 3]};
        *(LAS f32x4*)(ob + (unsigned)kh * 128u + g * 32) = va;
        *(LAS f32x4*)(ob + (unsigned)(kh ^ 1) * 128u + g * 32) = vb;
      }
    }
    BAR();
    STAMP(15);
  } else {
    const int hw = wave - 4;
    auto w3blk = [&](int blk, unsigned dst) {
#pragma unroll 1
      for (int p = 0; p < 2; ++p) {
        const unsigned row0 = (unsigned)(32 * hw + 16 * p), so = (128u * blk + row0) * 2752u + 64u * du0, ld = LB + dst + row0 * 192u;
#pragma unroll
        for (int i = 0; i < 3; ++i) {
          dma16(rWG, v3g[i], so, ld + i * 1024u);
          dma16(rWU, v3u[i], so, ld + 24576u + i * 1024u);
        }
      }
    };
    VMWAIT(0);
    signal(cntT, lane);
    RBAR();
    w3blk(1, 49152);
    u32x4 b3[12];
#pragma unroll
    for (int q = 0; q < 12; ++q) {
      const unsigned so = (384u + (unsigned)(32 * hw + 16 * (q / 6))) * 2752u + 64u * du0;
      if ((q % 6) < 3) asm volatile("buffer_load_dwordx4 %0, %1, %2, %3 offen" : "=v"(b3[q]) : "v"(v3g[q % 3]), "s"(rWG), "s"(so) : "memory");
      else asm volatile("buffer_load_dwordx4 %0, %1, %2, %3 offen" : "=v"(b3[q]) : "v"(v3u[q % 3]), "s"(rWU), "s"(so) : "memory");
    }
    VMWAIT(12);
    RBAR();
    RBAR();
    RBAR();
    w3blk(2, 98304);
    asm volatile("s_waitcnt vmcnt(0)" : "+v"(b3[0]), "+v"(b3[1]), "+v"(b3[2]), "+v"(b3[3]), "+v"(b3[4]), "+v"(b3[5]), "+v"(b3[6]), "+v"(b3[7]), "+v"(b3[8]), "+v"(b3[9]), "+v"(b3[10]), "+v"(b3[11])::"memory");
    if (hw == (G & 3)) {
      const int blk = G >> 2;
      if (blk == 3) {
#pragma unroll
        for (int q = 0; q < 12; ++q) ldsst(LB + L_SCR + (unsigned)(16 * (q / 6)) * 192u + ((q % 6) >= 3 ? 6144u : 0u) + (unsigned)(q % 3) * 1024u + lane * 16, b3[q]);
        asm volatile("s_waitcnt lgkmcnt(0)" ::: "memory");
      }
      const unsigned we = LB + (blk == 3 ? L_SCR + (unsigned)trcb * 6144u : (unsigned)(blk * 49152 + trcb * 24576 + 32 * hw * 192)) + (unsigned)((4 * h + trq) * 192 + trp * 8 + trcb * 32);
      const unsigned wo = we - (unsigned)(trcb * 64);
      f32x16 cs;
#pragma unroll
      for (int i = 0; i < 16; ++i) cs[i] = 0.f;
      bf16x8 f[12];
#pragma unroll
      for (int q = 0; q < 12; ++q) { const int u = q >> 1; const unsigned a = ((u & 1) ? wo : we) + (q & 1) * 3072 + u * 32; f[q] = trfrag(a, a + 1536); }
#pragma unroll
      for (int q = 0; q < 12; ++q) {
        const unsigned sel = (r == (q >> 1)) ? 0x3F803F80u : 0u;
        const u32x4 sv = {sel, sel, sel, sel};
        cs = MFMA(f[q], asfrag(sv), cs);
      }
      if (r < 6) {
        const unsigned tg32 = (unsigned)T;
#pragma unroll
        for (int g = 0; g < 16; ++g)
          __hip_atomic_store((GAS u64*)(ws + OFF_CS2 + (unsigned)((j * 16 + G) * 1536 + (r * 32 + (g >> 2) * 8 + h * 4 + (g & 3)) * 8)), ((u64)tg32 << 32) | __float_as_uint(cs[g]), RLX_AGENT);
      }
    }
    if (b == 0 && hw == 3) {
      unsigned spins = 0;
#pragma unroll 1
      for (;;) {
        bool ok = true;
#pragma unroll
        for (int i = 0; i < 4; ++i) ok &= __hip_atomic_load((const GAS u64*)(ws + OFF_KF + (unsigned)(i * 64 + lane) * 128u), RLX_AGENT) == T;
        if (__all(ok)) break;
        __builtin_amdgcn_s_sleep(2);
        if (++spins > SPIN_CAP) break;
      }
      if (lane == 0) __hip_atomic_store((GAS u64*)(ws + OFF_EPOCH), T, RLX_AGENT);
    }
    STAMP(20);
    RBAR();
#pragma unroll
    for (int q = 0; q < 12; ++q)
      ldsst(LB + (unsigned)(32 * hw + 16 * (q / 6)) * 192u + ((q % 6) >= 3 ? 24576u : 0u) + (unsigned)(q % 3) * 1024u + lane * 16, b3[q]);
    STAMP(21);
    BAR();
    if (hw < 3) {
      float pv[16];
      unsigned spins = 0;
#pragma unroll 1
      for (;;) {
        bool ok = true;
#pragma unroll
        for (int g = 0; g < 16; ++g) {
          const u64 x = __hip_atomic_load((const GAS u64*)(ws + OFF_CS2 + (unsigned)((j * 16 + g) * 1536 + (hw * 64 + lane) * 8)), RLX_AGENT);
          pv[g] = __uint_as_float((unsigned)x);
          ok &= (unsigned)(x >> 32) == (unsigned)T;
        }
        if (__all(ok)) break;
        __builtin_amdgcn_s_sleep(2);
        if (++spins > SPIN_CAP) break;
      }
      const float v = (((pv[0] + pv[1]) + (pv[2] + pv[3])) + ((pv[4] + pv[5]) + (pv[6] + pv[7]))) + (((pv[8] + pv[9]) + (pv[10] + pv[11])) + ((pv[12] + pv[13]) + (pv[14] + pv[15])));
      const unsigned c1 = pk2(v, 0.f) & 0xFFFFu;
      const float r1 = v - blo(c1);
      const unsigned c2 = pk2(r1, 0.f) & 0xFFFFu;
      const unsigned c3 = pk2(r1 - blo(c2), 0.f) & 0xFFFFu;
      const u32x4 f = {c1 | (c1 << 16), c2 | (c1 << 16), c2 | (c3 << 16), 0u};
      ldsst(LB + L_SCR + (unsigned)(hw * 64 + lane) * 16u, f);
    }
    BAR();
    auto w4 = [&](int m0, int n, unsigned dst) {
#pragma unroll 1
      for (int q = 0; q < n; ++q) {
        const int mm = hw + 4 * q;
        dma_run4(rWD, v1k, (unsigned)Dm(m0 + mm) * 32768u + 128u * c, 8192, LB + dst + (unsigned)mm * 4096u, 1);
      }
    };
    w4(0, 3, 49152);
    w4(12, 3, 0);
    RBAR();
    w4(24, 3, 98304);
    wait_cnt(cntT + 96 + 32 * hp, 0u, hp ? 11u : 16u);
    VMWAIT(0);
    RBAR();
    RBAR();
    w4(36, 2, 49152);
    VMWAIT(8);
    RBAR();
    VMWAIT(0);
    RBAR();
    RBAR();
    RBAR();
  }
  {
    const int row = threadIdx.x >> 3, ch = threadIdx.x & 7;
    const unsigned a = LB + (unsigned)row * 272u + (unsigned)ch * 32u;
    const f32x4 p0 = *(LAS f32x4*)a + *(LAS f32x4*)(a + 17408), p1 = *(LAS f32x4*)(a + 16) + *(LAS f32x4*)(a + 17408 + 16);
    const u32x4 o = {pk2(p0[0], p0[1]), pk2(p0[2], p0[3]), pk2(p1[0], p1[1]), pk2(p1[2], p1[3])};
    __builtin_amdgcn_raw_buffer_store_b128(o, rOUT, (unsigned)((64 * H + row) * 1024 + 128 * c + ch * 16), 0, 16);
  }
  STAMP_END();
}

extern "C" void kernel_launch(void* const* d_in, const int* in_sizes, int n_in, void* d_out, int out_size, void* d_ws, size_t ws_size,
                              hipStream_t stream) {
  (void)in_sizes; (void)n_in; (void)out_size; (void)ws_size;
  (void)hipFuncSetAttribute((const void*)fused_block, hipFuncAttributeMaxDynamicSharedMemorySize, (int)LDS_BYTES);
  hipLaunchKernelGGL(fused_block, dim3(256), dim3(512), LDS_BYTES, stream, d_in[0], d_in[1], d_in[2], d_in[4], d_in[5], d_in[6], d_out, (char*)d_ws);
}
```

```cpp
#include <hip/hip_runtime.h>
#include <hip/hip_bf16.h>
#include <utility>

typedef __bf16 bf16x8 __attribute__((ext_vector_type(8)));
typedef __bf16 bf16x4 __attribute__((ext_vector_type(4)));
typedef __bf16 bf16x2 __attribute__((ext_vector_type(2)));
typedef float f32x16 __attribute__((ext_vector_type(16)));
typedef float f32x4 __attribute__((ext_vector_type(4)));
typedef float f32x2 __attribute__((ext_vector_type(2)));
typedef unsigned u32x4 __attribute__((ext_vector_type(4)));
typedef unsigned u32x2 __attribute__((ext_vector_type(2)));
typedef unsigned long long u64;
#define LAS __attribute__((address_space(3)))
#define GAS __attribute__((address_space(1)))
#define DI __device__ __forceinline__
#define RLX_AGENT __ATOMIC_RELAXED, __HIP_MEMORY_SCOPE_AGENT
#define MFMA(a, b, c) __builtin_amdgcn_mfma_f32_32x32x16_bf16((a), (b), (c), 0, 0, 0)
#define MFMAP(acc, a, b) do { acc = MFMA(a, b, acc); asm volatile("" : "+v"(acc)); } while (0)
#define OPQ(x) asm volatile("" : "+v"(x))
typedef __amdgpu_buffer_rsrc_t rsrc_t;

constexpr unsigned OFF_EPOCH = 0;
constexpr unsigned OFF_READY = 4096;
constexpr unsigned OFF_KF = 16384;
constexpr unsigned OFF_VF = OFF_KF + 32768;
constexpr unsigned OFF_CNT = OFF_VF + 32768;
constexpr unsigned OFF_CSF = 192u << 10;
constexpr unsigned OFF_CS = 200u << 10;
constexpr unsigned OFF_CS2 = 9u * (1u << 20) + (512u << 10);
constexpr unsigned OFF_STAMP = 512u << 10;
constexpr unsigned OFF_K = 1u << 20;
constexpr unsigned OFF_V = 3u << 20;
constexpr unsigned OFF_O = 5u << 20;
constexpr unsigned OFF_H = 7u << 20;
constexpr unsigned OFF_HS = 9u << 20;
constexpr unsigned OFF_HH = 10u << 20;
constexpr unsigned WS_BYTES = 16u << 20;
constexpr unsigned KB = 1024;
constexpr unsigned L_WK = 0, L_WV = 64 * KB, L_XLN = 96 * KB, L_WQA = 128 * KB, L_WQB = 32 * KB;
constexpr unsigned L_K = 0, L_V = 48 * KB, L_QH = 40 * KB, L_ONES = 88 * KB, L_WO = 96 * KB;
constexpr unsigned L_SCR = 144 * KB;
constexpr unsigned LDS_BYTES = 160 * KB;
constexpr unsigned SPIN_CAP = 1u << 18;

#define BAR() do { asm volatile("s_waitcnt lgkmcnt(0)" ::: "memory"); __builtin_amdgcn_s_barrier(); asm volatile("" ::: "memory"); } while (0)
#define VMWAIT(n) asm volatile("s_waitcnt vmcnt(" #n ")" ::: "memory")

#define STAMP(i) do {} while (0)
#define STAMP_END() do {} while (0)

DI unsigned rfl(unsigned x) { return (unsigned)__builtin_amdgcn_readfirstlane((int)x); }
DI void dma16(rsrc_t rs, unsigned voff, unsigned soff, unsigned lds) {
  asm volatile("s_mov_b32 m0, %0\n\ts_nop 0\n\tbuffer_load_dwordx4 %1, %2, %3 offen lds" ::"s"(lds), "v"(voff), "s"(rs), "s"(soff) : "m0", "memory");
}
DI void dma16_sc1(rsrc_t rs, unsigned voff, unsigned soff, unsigned lds) {
  asm volatile("s_mov_b32 m0, %0\n\ts_nop 0\n\tbuffer_load_dwordx4 %1, %2, %3 offen sc1 lds" ::"s"(lds), "v"(voff), "s"(rs), "s"(soff) : "m0", "memory");
}
DI void dma_run4(rsrc_t rs, unsigned voff, unsigned soff, unsigned step, unsigned lds, int n4) {
#pragma unroll 1
  for (int i = 0; i < n4; ++i) {
    asm volatile(
        "s_mov_b32 m0, %[l]\n\ts_nop 0\n\tbuffer_load_dwordx4 %[v], %[r], %[s] offen lds\n\t"
        "s_add_u32 m0, m0, 0x400\n\ts_add_u32 %[s], %[s], %[st]\n\tbuffer_load_dwordx4 %[v], %[r], %[s] offen lds\n\t"
        "s_add_u32 m0, m0, 0x400\n\ts_add_u32 %[s], %[s], %[st]\n\tbuffer_load_dwordx4 %[v], %[r], %[s] offen lds\n\t"
        "s_add_u32 m0, m0, 0x400\n\ts_add_u32 %[s], %[s], %[st]\n\tbuffer_load_dwordx4 %[v], %[r], %[s] offen lds\n\t"
        "s_add_u32 %[s], %[s], %[st]\n\ts_add_u32 %[l], %[l], 0x1000"
        : [s] "+s"(soff), [l] "+s"(lds) : [v] "v"(voff), [r] "s"(rs), [st] "s"(step) : "m0", "scc", "memory");
  }
}
DI void dma_run4_sc1(rsrc_t rs, unsigned voff, unsigned soff, unsigned lds) {
  asm volatile(
      "s_mov_b32 m0, %[l]\n\ts_nop 0\n\tbuffer_load_dwordx4 %[v], %[r], %[s] offen sc1 lds\n\t"
      "buffer_load_dwordx4 %[v], %[r], %[s] offen offset:1024 sc1 lds\n\t"
      "buffer_load_dwordx4 %[v], %[r], %[s] offen offset:2048 sc1 lds\n\t"
      "buffer_load_dwordx4 %[v], %[r], %[s] offen offset:3072 sc1 lds"
      ::[s] "s"(soff), [l] "s"(lds), [v] "v"(voff), [r] "s"(rs) : "m0", "memory");
}
#define RBAR() do { asm volatile("" ::: "memory"); __builtin_amdgcn_s_barrier(); asm volatile("" ::: "memory"); } while (0)
#define SCHED() __builtin_amdgcn_sched_barrier(0)
template <int... I, class F> DI void sfor_(std::integer_sequence<int, I...>, F&& f) { (f(std::integral_constant<int, I>{}), ...); }
template <int N, class F> DI void sfor(F&& f) { sfor_(std::make_integer_sequence<int, N>{}, f); }
template <int N, int D, class LD, class MM, class PRE>
DI void pipe(LD&& ld, MM&& mm, PRE&& pre) {
  bf16x8 buf[D];
  sfor<D>([&](auto I) { pre(I); buf[I.value] = ld(I); });
  SCHED();
  sfor<N>([&](auto S) {
    constexpr int s = S.value;
    mm(S, buf[s % D]);
    if constexpr (s + D < N) { pre(std::integral_constant<int, s + D>{}); buf[s % D] = ld(std::integral_constant<int, s + D>{}); }
    SCHED();
  });
}
DI bf16x8 trfrag(unsigned a0, unsigned a1) {
  bf16x4 lo = __builtin_amdgcn_ds_read_tr16_b64_v4bf16((LAS bf16x4*)a0);
  bf16x4 hi = __builtin_amdgcn_ds_read_tr16_b64_v4bf16((LAS bf16x4*)a1);
  return __builtin_shufflevector(lo, hi, 0, 1, 2, 3, 4, 5, 6, 7);
}
DI bf16x8 ldsfrag(unsigned a) { return *(LAS bf16x8*)a; }
DI void ldsst(unsigned a, u32x4 v) { *(LAS u32x4*)a = v; }
DI unsigned pk2(float a, float b) { f32x2 v = {a, b}; return __builtin_bit_cast(unsigned, __builtin_convertvector(v, bf16x2)); }
DI u32x4 pack8(const f32x16& x, int o) { u32x4 r; r[0] = pk2(x[o], x[o + 1]); r[1] = pk2(x[o + 2], x[o + 3]); r[2] = pk2(x[o + 4], x[o + 5]); r[3] = pk2(x[o + 6], x[o + 7]); return r; }
DI float blo(unsigned u) { return __uint_as_float(u << 16); }
DI float bhi(unsigned u) { return __uint_as_float(u & 0xffff0000u); }
DI bf16x8 asfrag(u32x4 v) { return __builtin_bit_cast(bf16x8, v); }
DI float swap32_add(float a) {
  auto r = __builtin_amdgcn_permlane32_swap(__float_as_uint(a), __float_as_uint(a), false, false);
  return __uint_as_float(r[0]) + __uint_as_float(r[1]);
}
DI float swap32_max(float a) {
  auto r = __builtin_amdgcn_permlane32_swap(__float_as_uint(a), __float_as_uint(a), false, false);
  return fmaxf(__uint_as_float(r[0]), __uint_as_float(r[1]));
}
template <int CTRL> DI float dpp_add(float a) { return a + __uint_as_float(__builtin_amdgcn_update_dpp(0, __float_as_uint(a), CTRL, 0xf, 0xf, true)); }
DI float row16_sum(float a) { a = dpp_add<0x128>(a); a = dpp_add<0x124>(a); a = dpp_add<0x122>(a); a = dpp_add<0x121>(a); return a; }
DI void red8(const float (&v)[8], float& A, float& B) {
  float t[4];
#pragma unroll
  for (int i = 0; i < 4; ++i) {
    auto r = __builtin_amdgcn_permlane32_swap(__float_as_uint(v[2 * i]), __float_as_uint(v[2 * i + 1]), false, false);
    t[i] = __uint_as_float(r[0]) + __uint_as_float(r[1]);
  }
  auto r0 = __builtin_amdgcn_permlane16_swap(__float_as_uint(t[0]), __float_as_uint(t[1]), false, false);
  auto r1 = __builtin_amdgcn_permlane16_swap(__float_as_uint(t[2]), __float_as_uint(t[3]), false, false);
  A = row16_sum(__uint_as_float(r0[0]) + __uint_as_float(r0[1]));
  B = row16_sum(__uint_as_float(r1[0]) + __uint_as_float(r1[1]));
}
DI float rdl(float a, int l) { return __uint_as_float((unsigned)__builtin_amdgcn_readlane((int)__float_as_uint(a), l)); }
DI void bc8(float A, float B, float (&o)[8]) {
  o[0] = rdl(A, 0); o[2] = rdl(A, 16); o[1] = rdl(A, 32); o[3] = rdl(A, 48);
  o[4] = rdl(B, 0); o[6] = rdl(B, 16); o[5] = rdl(B, 32); o[7] = rdl(B, 48);
}
DI void wait_cnt(GAS unsigned* c, unsigned have, unsigned target) {
  unsigned spins = 0;
#pragma unroll 1
  while (have < target) {
    __builtin_amdgcn_s_sleep(4);
    have = rfl(__hip_atomic_load(c, RLX_AGENT));
    if (++spins > SPIN_CAP) break;
  }
  __builtin_amdgcn_fence(__ATOMIC_ACQUIRE, "wavefront");
}
DI unsigned arrive(GAS unsigned* c, int lane) {
  unsigned r = 0;
  if (lane == 0) r = __hip_atomic_fetch_add(c, 1u, RLX_AGENT) + 1u;
  r = rfl(r);
  asm volatile("" ::"s"(r));
  return r;
}

__host__ __device__ constexpr int Dm(int m) { return m < 32 ? ((43 * (m & 15)) >> 4) + (m >> 4) : (4 + 8 * ((m - 32) >> 1) + 3 * ((m - 32) & 1) < 42 ? 4 + 8 * ((m - 32) >> 1) + 3 * ((m - 32) & 1) : 42); }

template <bool FULL, class MID, class HOOK, class ST>
DI u32x4 attention(const unsigned ka, const unsigned va, const bf16x8 q, const int n0, const int e, const int r, const int h, const bool helper, MID&& mid, HOOK&& hook, ST&& stamp) {
  bf16x8 kf[9];
#pragma unroll
  for (int n = 0; n < 9; ++n) kf[n] = ldsfrag(ka + n * 4096);
  f32x16 zero;
#pragma unroll
  for (int i = 0; i < 16; ++i) zero[i] = 0.f;
  f32x16 sm = MFMA(kf[8], q, zero);
  f32x16 sa, sb;
  if (FULL) {
    sb = MFMA(kf[0], q, zero);
    sa = MFMA(kf[1], q, zero);
    OPQ(sa);
  } else {
#pragma unroll
    for (int i = 0; i < 16; ++i) sb[i] = -1e30f;
  }
#pragma unroll
  for (int i = 0; i < 16; ++i) { const int kl = (i & 3) + 8 * (i >> 2) + 4 * h; sm[i] = (kl <= r) ? sm[i] : sb[i]; }
  float mx = sm[0], mx2 = sm[1];
#pragma unroll
  for (int i = 2; i < 16; i += 2) mx = fmaxf(fmaxf(mx, sm[i]), sm[i + 1]);
  if (FULL) {
    SCHED();
#pragma unroll
    for (int n = 1; n < 8; ++n) {
      if (n & 1) {
        if (n < 7) { sb = MFMA(kf[n + 1], q, zero); OPQ(sb); }
        SCHED();
#pragma unroll
        for (int i = 0; i < 16; i += 4) { mx = fmaxf(fmaxf(mx, sa[i]), sa[i + 1]); mx2 = fmaxf(fmaxf(mx2, sa[i + 2]), sa[i + 3]); }
      } else {
        sa = MFMA(kf[n + 1], q, zero); OPQ(sa);
        SCHED();
#pragma unroll
        for (int i = 0; i < 16; i += 4) { mx = fmaxf(fmaxf(mx, sb[i]), sb[i + 1]); mx2 = fmaxf(fmaxf(mx2, sb[i + 2]), sb[i + 3]); }
      }
      SCHED();
      if (n == 3) mid();
      if (n >= 4) hook(4 + n);
    }
  } else {
    mid();
#pragma unroll
    for (int n = 8; n < 12; ++n) hook(n);
#pragma unroll
    for (int n = 1; n < 8; ++n) {
      if (n >= n0) {
        const f32x16 s = MFMA(kf[n], q, zero);
#pragma unroll
        for (int i = 0; i < 16; ++i) mx = fmaxf(mx, s[i]);
      }
    }
  }
  mx = swap32_max(fmaxf(mx, mx2));
  f32x16 negm;
#pragma unroll
  for (int i = 0; i < 16; ++i) negm[i] = -mx;
  if (FULL) { sa = MFMA(kf[1], q, negm); OPQ(sa); }
  u32x4 p0, p1, d0, d1;
  {
    f32x16 p;
#pragma unroll
    for (int i = 0; i < 16; ++i) p[i] = __builtin_amdgcn_exp2f(sm[i] - mx);
    p0 = pack8(p, 0); p1 = pack8(p, 8);
#pragma unroll
    for (int k = 0; k < 4; ++k) {
      const int i0 = 2 * k, i1 = 8 + 2 * k;
      const int ka0 = (i0 & 3) + 8 * (i0 >> 2) + 4 * h, ka1 = (i1 & 3) + 8 * (i1 >> 2) + 4 * h;
      const unsigned m0 = (ka0 <= r ? 0xFFFFu : 0u) | (ka0 + 1 <= r ? 0xFFFF0000u : 0u);
      const unsigned m1 = (ka1 <= r ? 0xFFFFu : 0u) | (ka1 + 1 <= r ? 0xFFFF0000u : 0u);
      d0[k] = p0[k] & m0; d1[k] = p1[k] & m1;
    }
    p0 ^= d0; p1 ^= d1;
  }
  OPQ(p0); OPQ(p1); OPQ(d0); OPQ(d1);
  stamp(16);
  if (helper) VMWAIT(8);
  RBAR();
  stamp(7);
  const bool ones = ((r >> 4) != e);
  unsigned vp = ones ? L_ONES : va;
  const unsigned i1k = ones ? 0u : 1024u, i3k = ones ? 0u : 3072u;
  f32x16 o = zero;
  if (FULL) {
    const unsigned v8 = vp + 32 * i1k;
    const bf16x8 w80 = ldsfrag(v8), w81 = ldsfrag(v8 + i1k);
    const bf16x8 w00 = ldsfrag(vp); vp += i1k;
    const bf16x8 w01 = ldsfrag(vp); vp += i3k;
    bf16x8 v0 = ldsfrag(vp); vp += i1k;
    bf16x8 v1 = ldsfrag(vp); vp += i3k;
    o = MFMA(w80, asfrag(d0), o);
    o = MFMA(w81, asfrag(d1), o);
    o = MFMA(w00, asfrag(p0), o);
    o = MFMA(w01, asfrag(p1), o);
    OPQ(o);
    SCHED();
#pragma unroll
    for (int n = 1; n < 8; ++n) {
      bf16x8 n0f, n1f;
      if (n < 7) {
        if (n & 1) { sb = MFMA(kf[n + 1], q, negm); OPQ(sb); } else { sa = MFMA(kf[n + 1], q, negm); OPQ(sa); }
        n0f = ldsfrag(vp); vp += i1k;
        n1f = ldsfrag(vp); vp += i3k;
        hook(n - 1);
      }
      SCHED();
      f32x16& s = (n & 1) ? sa : sb;
#pragma unroll
      for (int i = 0; i < 8; ++i) s[i] = __builtin_amdgcn_exp2f(s[i]);
      MFMAP(o, v0, asfrag(pack8(s, 0)));
      SCHED();
#pragma unroll
      for (int i = 8; i < 16; ++i) s[i] = __builtin_amdgcn_exp2f(s[i]);
      MFMAP(o, v1, asfrag(pack8(s, 8)));
      SCHED();
      v0 = n0f; v1 = n1f;
    }
  } else {
#pragma unroll
    for (int n = 0; n < 6; ++n) hook(n);
    o = MFMA(ldsfrag(vp + 32 * i1k), asfrag(d0), o);
    o = MFMA(ldsfrag(vp + 33 * i1k), asfrag(d1), o);
#pragma unroll
    for (int n = 1; n < 8; ++n) {
      if (n >= n0) {
        f32x16 p = MFMA(kf[n], q, negm);
#pragma unroll
        for (int i = 0; i < 16; ++i) p[i] = __builtin_amdgcn_exp2f(p[i]);
        o = MFMA(ldsfrag(vp + (4 * n) * i1k), asfrag(pack8(p, 0)), o);
        o = MFMA(ldsfrag(vp + (4 * n + 1) * i1k), asfrag(pack8(p, 8)), o);
      }
    }
  }
  u32x4 of;
  if (e == 0) {
    const float il = __builtin_amdgcn_rcpf(o[8]);
#pragma unroll
    for (int i = 0; i < 8; ++i) o[i] *= il;
    of = pack8(o, 0);
    OPQ(of);
  } else {
    const float il = __builtin_amdgcn_rcpf(o[0]);
#pragma unroll
    for (int i = 8; i < 16; ++i) o[i] *= il;
    of = pack8(o, 8);
    OPQ(of);
  }
  return of;
}

DI void ln8(const u32x4* xr, const unsigned wb, const int lane) {
  float s1[8], s2[8], rs[8], nm[8];
  const bf16x2 one2 = __builtin_bit_cast(bf16x2, 0x3F803F80u);
#pragma unroll
  for (int i = 0; i < 8; ++i) {
    float a = 0.f, q = 0.f;
#pragma unroll
    for (int k = 0; k < 4; ++k) {
      const unsigned xu = xr[i][k];
      const bf16x2 v = __builtin_bit_cast(bf16x2, xu);
      a = __builtin_amdgcn_fdot2_f32_bf16(v, one2, a, false);
      q = __builtin_amdgcn_fdot2_f32_bf16(v, v, q, false);
    }
    s1[i] = a; s2[i] = q;
  }
  float A1, B1, A2, B2;
  red8(s1, A1, B1);
  red8(s2, A2, B2);
  A1 *= (1.f / 512.f); B1 *= (1.f / 512.f);
  A2 = __builtin_amdgcn_rsqf(fmaxf(fmaf(-A1, A1, A2 * (1.f / 512.f)), 0.f) + 1e-5f);
  B2 = __builtin_amdgcn_rsqf(fmaxf(fmaf(-B1, B1, B2 * (1.f / 512.f)), 0.f) + 1e-5f);
  bc8(A2, B2, rs);
  bc8(-A1 * A2, -B1 * B2, nm);
#pragma unroll
  for (int i = 0; i < 8; ++i) {
    u32x4 o;
#pragma unroll
    for (int k = 0; k < 4; ++k) o[k] = pk2(fmaf(blo(xr[i][k]), rs[i], nm[i]), fmaf(bhi(xr[i][k]), rs[i], nm[i]));
    ldsst(wb + (unsigned)((i ^ (lane & 7)) << 4), o);
  }
}
DI void poll_tag(const GAS u64* p, const u64 T) {
  unsigned spins = 0;
#pragma unroll 1
  for (;;) {
    const u64 v = __hip_atomic_load(p, RLX_AGENT);
    if (__all(v == T)) break;
    __builtin_amdgcn_s_sleep(1);
    if (++spins > SPIN_CAP) break;
  }
  asm volatile("" ::: "memory");
}
DI void signal(GAS unsigned* c, int lane) { if (lane == 0) (void)__hip_atomic_fetch_add(c, 1u, RLX_AGENT); }

__global__ void __launch_bounds__(512, 2)
fused_block(const void* __restrict__ gx, const void* __restrict__ gwqkv, const void* __restrict__ gwo, const void* __restrict__ gwg,
            const void* __restrict__ gwu, const void* __restrict__ gwd, void* __restrict__ gout, char* ws) {
  extern __shared__ __attribute__((aligned(16))) char smem[];
  const int lane = threadIdx.x & 63;
  const int wave = (int)rfl(threadIdx.x >> 6);
  STAMP(0);
  const int b = blockIdx.x, xcd = b & 7, idx = b >> 3;
  const int G = xcd * 2 + (idx >> 4), j = idx & 15, t = j >> 3, c = j & 7, H = 2 * G + t;
  const int r = lane & 31, h = lane >> 5;
  constexpr unsigned LB = 0;
  asm volatile("" ::"v"(smem));
  const rsrc_t rX = __builtin_amdgcn_make_buffer_rsrc((void*)gx, 0, 2048 * 512 * 2, 0x00020000);
  const rsrc_t rWQKV = __builtin_amdgcn_make_buffer_rsrc((void*)gwqkv, 0, 512 * 1536 * 2, 0x00020000);
  const rsrc_t rWO = __builtin_amdgcn_make_buffer_rsrc((void*)gwo, 0, 512 * 512 * 2, 0x00020000);
  const rsrc_t rWG = __builtin_amdgcn_make_buffer_rsrc((void*)gwg, 0, 512 * 1376 * 2, 0x00020000);
  const rsrc_t rWU = __builtin_amdgcn_make_buffer_rsrc((void*)gwu, 0, 512 * 1376 * 2, 0x00020000);
  const rsrc_t rWD = __builtin_amdgcn_make_buffer_rsrc((void*)gwd, 0, 1376 * 512 * 2, 0x00020000);
  const rsrc_t rOUT = __builtin_amdgcn_make_buffer_rsrc(gout, 0, 2048 * 512 * 2, 0x00020000);
  const rsrc_t rWS = __builtin_amdgcn_make_buffer_rsrc((void*)ws, 0, WS_BYTES, 0x00020000);

  const unsigned chsw = (unsigned)(((lane & 7) ^ (((lane >> 4) & 1) << 2)) * 16);
  const unsigned v1k = (unsigned)(lane >> 3) * 1024u + chsw;
  const int trq = (lane & 15) >> 2, trp = lane & 3, trcb = (lane >> 4) & 1;
  const int tau = (wave & 3) >> 1, hp = wave & 1;
  const int tt = 2 * H + tau;
  GAS unsigned* cntT = (GAS unsigned*)(ws + OFF_CNT + (unsigned)tt * 1024u);
  u32x4 qf;
  u32x2 xres[4];
  u64 T;

  const unsigned vq = (unsigned)(lane >> 3) * 3072u + chsw;
  {
    asm volatile("s_load_dwordx2 %0, %1, 0x0" : "=s"(T) : "s"(ws));
    if (j == 0 && wave == 4 && lane < 32) __hip_atomic_store((GAS unsigned*)(ws + OFF_CNT + (unsigned)(32 * G + lane) * 128u), 0u, RLX_AGENT);
    u32x4 xr[8];
    const unsigned vo = lane * 16, so = (unsigned)(64 * H + 8 * wave) * 1024u;
#pragma unroll
    for (int i = 0; i < 8; ++i)
      asm volatile("buffer_load_dwordx4 %0, %1, %2, %3 offen offset:%4" : "=v"(xr[i]) : "v"(vo + (i >> 2) * 4096), "s"(rX), "s"(so), "i"((i & 3) * 1024) : "memory");
    {
      const unsigned m0v = L_WK + (unsigned)wave * 8192u;
      if (wave == 6) {
        u64 pc;
        asm volatile("s_getpc_b64 %0" : "=s"(pc));
        const u64 a = (pc & ~127ull) + (u64)((idx * 8 + (lane & 7)) * 128);
        asm volatile("s_mov_b32 m0, %1\n\ts_nop 0\n\tglobal_load_lds_dword %0, off" ::"v"(a), "s"(m0v) : "m0", "memory");
      } else {
        const int n = wave == 0 ? 48 : wave == 5 ? 8 : 1;
        const int ln = lane < n ? lane : 0;
        const unsigned po = wave == 5 ? (unsigned)ln * (2u << 20) + (1u << 20) : wave == 7 ? (unsigned)(32 * tt) * 1024u : (unsigned)(b * n + ln) * 128u;
        const rsrc_t rp = wave == 0 ? rWQKV : wave == 1 ? rWG : wave == 2 ? rWU : wave == 3 ? rWD : wave == 4 ? rWO : wave == 5 ? rWS : rOUT;
        asm volatile("s_mov_b32 m0, %2\n\ts_nop 0\n\tbuffer_load_dword %0, %1, 0 offen lds" ::"v"(po), "s"(rp), "s"(m0v) : "m0", "memory");
      }
    }
#define PFN 1
    RBAR();
    dma_run4(rWQKV, vq, (unsigned)(64 * wave) * 3072u + (512u + 64u * c) * 2u, 8 * 3072, LB + L_WK + (unsigned)(64 * wave) * 128u, 2);
    asm volatile("s_waitcnt vmcnt(%9) lgkmcnt(0)" : "+v"(xr[0]), "+v"(xr[1]), "+v"(xr[2]), "+v"(xr[3]), "+v"(xr[4]), "+v"(xr[5]), "+v"(xr[6]), "+v"(xr[7]), "+s"(T) : "i"(8 + PFN) : "memory");
    T += 1ull;
    STAMP(1);
    ln8(xr, LB + L_XLN + (unsigned)(wave >> 2) * 32768u + (unsigned)(lane >> 1) * 1024u + (unsigned)(lane & 1) * 512u + (unsigned)(wave & 3) * 128u, lane);
    STAMP(30);
  }
  const int Hp = H - 8 + wave;

  if (wave < 4) {
    {
      const int n = wave == 3 ? 16 : 43;
      const unsigned po = (unsigned)(b * n + (lane < n ? lane : 0)) * 128u;
      const rsrc_t rp = wave == 0 ? rWG : wave == 1 ? rWU : wave == 2 ? rWD : rWO;
      asm volatile("s_mov_b32 m0, %2\n\ts_nop 0\n\tbuffer_load_dword %0, %1, 0 offen lds" ::"v"(po), "s"(rp), "s"(L_WV + (unsigned)wave * 8192u) : "m0", "memory");
    }
    dma_run4(rWQKV, vq, (unsigned)(64 * wave) * 3072u + (1024u + 64u * c) * 2u, 8 * 3072, LB + L_WV + (unsigned)(64 * wave) * 128u, 2);
    VMWAIT(9);
    BAR();
    STAMP(2);
    const unsigned trn = LB + (unsigned)((8 * h + trq) * 128 + ((hp ^ (trq >> 1)) * 64) + trcb * 32 + trp * 8);
    unsigned trn2 = trn + 65536u, trn3 = trn + L_WQA;
    OPQ(trn2); OPQ(trn3);
    bf16x8 xf[32];
    const unsigned xb = LB + L_XLN + (unsigned)tau * 32768u + (unsigned)h * 512u;
    auto ldx = [&](int s) { xf[s] = ldsfrag(xb + (unsigned)s * 1024u + (unsigned)((r ^ ((2 * (s & 3) + h) & 7)) << 4)); };
    f32x16 aK, aV, aQ;
#pragma unroll
    for (int i = 0; i < 16; ++i) { aK[i] = 0.f; aV[i] = 0.f; aQ[i] = 0.f; }
    u32x4 kf0, kf1, vf0, vf1;
#pragma unroll
    for (int s = 0; s < 4; ++s) ldx(s);
    const unsigned kvo = (unsigned)((tt * 8 + c) * 4 + hp * 2) * 1024u + lane * 16;
    pipe<32, 4>([&](auto I_) __attribute__((always_inline)) { constexpr int i = I_.value; return trfrag(trn + i * 2048, trn + i * 2048 + 512); },
                [&](auto I_, const bf16x8& w) __attribute__((always_inline)) {
                  constexpr int i = I_.value;
                  MFMAP(aK, w, xf[i]);
                  if constexpr (i + 4 < 32) ldx(i + 4);
                },
                [&](auto) {});
    kf0 = pack8(aK, 0); kf1 = pack8(aK, 8);
    __builtin_amdgcn_raw_buffer_store_b128(kf0, rWS, OFF_K + kvo, 0, 16);
    __builtin_amdgcn_raw_buffer_store_b128(kf1, rWS, OFF_K + kvo + 1024, 0, 16);
    STAMP(3);
    VMWAIT(0);
    if (lane == 0) __hip_atomic_store((GAS u64*)(ws + OFF_KF + (unsigned)(H * 8 + c) * 128u) + wave, T, RLX_AGENT);
    RBAR();
    STAMP(27);
    pipe<32, 5>([&](auto I_) __attribute__((always_inline)) { constexpr int i = I_.value; return trfrag(trn2 + i * 2048, trn2 + i * 2048 + 512); },
                [&](auto I_, const bf16x8& w) __attribute__((always_inline)) { MFMAP(aV, xf[I_.value], w); },
                [&](auto I_) __attribute__((always_inline)) { if (I_.value == 16) RBAR();   });
    vf0 = pack8(aV, 0); vf1 = pack8(aV, 8);
    __builtin_amdgcn_raw_buffer_store_b128(vf0, rWS, OFF_V + kvo, 0, 16);
    __builtin_amdgcn_raw_buffer_store_b128(vf1, rWS, OFF_V + kvo + 1024, 0, 16);
    STAMP(4);
    RBAR();
    pipe<32, 5>(
        [&](auto I_) __attribute__((always_inline)) {
          constexpr int i = I_.value;
          const unsigned a = i < 16 ? trn3 + i * 2048 : trn + L_WQB + (i - 16) * 2048;
          return trfrag(a, a + 512);
        },
        [&](auto I_, const bf16x8& w) __attribute__((always_inline)) {
          constexpr int i = I_.value;
          MFMAP(aQ, w, xf[i]);
          if (i == 18) {
            VMWAIT(0);
            if (lane == 0) __hip_atomic_store((GAS u64*)(ws + OFF_VF + (unsigned)(H * 8 + c) * 128u) + wave, T, RLX_AGENT);
          }
        },
        [&](auto) {});
    STAMP(5);
    RBAR();
#pragma unroll
    for (int i = 0; i < 16; ++i) aQ[i] *= 0.25f * 1.44269504088896f;
    qf = pack8(aQ, 0);
    const u32x4 qf1 = pack8(aQ, 8);
    ldsst(LB + L_K + (unsigned)((8 + tau) * 4 + hp * 2) * 1024u + lane * 16, kf0);
    ldsst(LB + L_K + (unsigned)((8 + tau) * 4 + hp * 2 + 1) * 1024u + lane * 16, kf1);
    ldsst(LB + L_V + (unsigned)((8 + tau) * 4 + hp * 2) * 1024u + lane * 16, vf0);
    ldsst(LB + L_V + (unsigned)((8 + tau) * 4 + hp * 2 + 1) * 1024u + lane * 16, vf1);
    ldsst(LB + L_QH + (unsigned)wave * 1024u + lane * 16, qf1);
    { const u32x4 one4 = {0x3F803F80u, 0x3F803F80u, 0x3F803F80u, 0x3F803F80u}; ldsst(LB + L_ONES, one4); }
#pragma unroll
    for (int g = 0; g < 4; ++g)
      xres[g] = __builtin_amdgcn_raw_buffer_load_b64(rX, (unsigned)((32 * tt + r) * 1024 + (64 * c + 32 * hp + 8 * g + 4 * h) * 2), 0, 0);
    BAR();
  } else {
    const int hw = wave - 4;
    VMWAIT(0);
    BAR();
    if (j == 0 && hw == 0 && lane == 0) __hip_atomic_store((GAS u64*)(ws + OFF_READY + (unsigned)G * 256u), T, RLX_AGENT);
    u32x4 st[16];
#pragma unroll
    for (int i = 0; i < 16; ++i) {
      const unsigned so = (unsigned)((i < 8 ? 256 : 0) + 64 * hw + 8 * (i & 7)) * 3072u + ((i < 8 ? 1024u : 0u) + 64u * c) * 2u;
      asm volatile("buffer_load_dwordx4 %0, %1, %2, %3 offen" : "=v"(st[i]) : "v"(vq), "s"(rWQKV), "s"(so) : "memory");
    }
    STAMP(3);
    RBAR();
    dma_run4(rWQKV, vq, (unsigned)(256 + 64 * hw) * 3072u + (64u * c) * 2u, 8 * 3072, LB + L_WQB + (unsigned)(64 * hw) * 128u, 2);
    asm volatile("s_waitcnt vmcnt(16)" : "+v"(st[0]), "+v"(st[1]), "+v"(st[2]), "+v"(st[3]), "+v"(st[4]), "+v"(st[5]), "+v"(st[6]), "+v"(st[7])::"memory");
#pragma unroll
    for (int i = 0; i < 8; ++i) ldsst(LB + L_WV + (unsigned)(256 + 64 * hw + 8 * i) * 128u + lane * 16, st[i]);
    BAR();
    asm volatile("s_waitcnt vmcnt(8)" : "+v"(st[8]), "+v"(st[9]), "+v"(st[10]), "+v"(st[11]), "+v"(st[12]), "+v"(st[13]), "+v"(st[14]), "+v"(st[15])::"memory");
#pragma unroll
    for (int i = 0; i < 8; ++i) ldsst(LB + L_WQA + (unsigned)(64 * hw + 8 * i) * 128u + lane * 16, st[8 + i]);
    VMWAIT(0);
    BAR();
    if (Hp >= 0) {
      poll_tag((const GAS u64*)(ws + OFF_KF + (unsigned)(Hp * 8 + c) * 128u) + (lane & 3), T);
      dma_run4_sc1(rWS, lane * 16, OFF_K + (unsigned)((2 * Hp) * 8 + c) * 4096u, LB + L_K + (unsigned)(8 * hw) * 1024u);
      dma_run4_sc1(rWS, lane * 16, OFF_K + (unsigned)((2 * Hp + 1) * 8 + c) * 4096u, LB + L_K + (unsigned)(8 * hw + 4) * 1024u);
    }
    STAMP(5);
    RBAR();
    VMWAIT(0);
    RBAR();
    qf = *(LAS u32x4*)(LB + L_QH + (unsigned)hw * 1024u + lane * 16);
#pragma unroll
    for (int g = 0; g < 4; ++g) { xres[g][0] = 0u; xres[g][1] = 0u; }
  }
  STAMP(6);

  const int du0 = (43 * j) >> 4, ndu = ((43 * (j + 1)) >> 4) - du0;
  unsigned v3g[3], v3u[3];
#pragma unroll
  for (int i = 0; i < 3; ++i) {
    const unsigned qd = (unsigned)(i * 64 + lane), row = qd / 12u, ch = qd - row * 12u;
    v3g[i] = row * 2752u + ch * 16u;
    v3u[i] = row * 2752u + (ch ^ 2u) * 16u;
  }
  {
    const int e = wave >> 2;
    const unsigned ka = LB + L_K + (unsigned)(tau * 4 + hp * 2 + e) * 1024u + lane * 16;
    const unsigned va = LB + L_V + (unsigned)(tau * 4 + hp * 2) * 1024u + lane * 16;
    const unsigned so0 = (unsigned)(16 * wave) * 2752u + 64u * du0, ld0 = LB + (unsigned)(16 * wave) * 192u;
    auto hook = [&](int n) {
      if (n < 3) { dma16(rWG, v3g[n], so0, ld0 + n * 1024u); dma16(rWU, v3u[n], so0, ld0 + 24576u + n * 1024u); }
      else if (n >= 8) {
        const unsigned row0 = (unsigned)(64 * wave + 16 * (n - 8));
        dma16(rWO, v1k, row0 * 1024u + 128u * c, LB + L_WO + row0 * 128u);
        dma16(rWO, v1k, (row0 + 8u) * 1024u + 128u * c, LB + L_WO + (row0 + 8u) * 128u);
      }
    };
    auto mid = [&]() {
      if (wave >= 4) {
        const int hw = wave - 4;
        poll_tag((lane == 4 || Hp < 0) ? (const GAS u64*)(ws + OFF_READY + (unsigned)G * 256u) : (const GAS u64*)(ws + OFF_VF + (unsigned)(Hp * 8 + c) * 128u) + (lane & 3), T);
        if (Hp >= 0) {
          dma_run4_sc1(rWS, lane * 16, OFF_V + (unsigned)((2 * Hp) * 8 + c) * 4096u, LB + L_V + (unsigned)(8 * hw) * 1024u);
          dma_run4_sc1(rWS, lane * 16, OFF_V + (unsigned)((2 * Hp + 1) * 8 + c) * 4096u, LB + L_V + (unsigned)(8 * hw + 4) * 1024u);
        }
      }
    };
    auto st = [&](int i) { STAMP(i); };
    u32x4 of;
    if (tt >= 8) of = attention<true>(ka, va, asfrag(qf), 0, e, r, h, wave >= 4, mid, hook, st);
    else of = attention<false>(ka, va, asfrag(qf), 8 - tt, e, r, h, wave >= 4, mid, hook, st);
    __builtin_amdgcn_raw_buffer_store_b128(of, rWS, OFF_O + (unsigned)(tt * 32 + 4 * c + 2 * hp + e) * 1024u + lane * 16, 0, 16);
  }
  STAMP(17);
  const unsigned trpm = (unsigned)((4 * h + trq) * 128 + trcb * 32 + trp * 8);

  if (wave < 4) {
    const int kh = hp;
    VMWAIT(0);
    const unsigned haveO = arrive(cntT, lane);
    RBAR();
    STAMP(8);
    wait_cnt(cntT, haveO, 32u);
    STAMP(9);
    u32x4 of[16];
#pragma unroll
    for (int s = 0; s < 16; ++s) of[s] = __builtin_amdgcn_raw_buffer_load_b128(rWS, OFF_O + (unsigned)(tt * 32 + 16 * kh + s) * 1024u + lane * 16, 0, 16);
    const unsigned aA = LB + L_WO + (unsigned)kh * 32768u + trpm + (unsigned)((kh ^ (trq >> 1)) * 64);
    const unsigned aB = aA ^ 64u;
    f32x16 accA, accB;
#pragma unroll
    for (int i = 0; i < 16; ++i) { accA[i] = 0.f; accB[i] = 0.f; }
    pipe<32, 6>([&](auto I_) { constexpr int i = I_.value; const unsigned a = ((i & 1) ? aA : aB) + (i >> 1) * 2048; return trfrag(a, a + 1024); },
                [&](auto I_, const bf16x8& w) { constexpr int i = I_.value; if (i & 1) MFMAP(accA, w, asfrag(of[i >> 1])); else MFMAP(accB, w, asfrag(of[i >> 1])); },
                [&](auto) {});
    STAMP(18);
    RBAR();
#pragma unroll
    for (int g = 0; g < 4; ++g) { f32x4 v = {accB[4 * g], accB[4 * g + 1], accB[4 * g + 2], accB[4 * g + 3]}; *(LAS f32x4*)(LB + L_SCR + (unsigned)wave * 4096u + g * 1024 + lane * 16) = v; }
    BAR();
    unsigned hres[8];
    {
      float hv[16];
#pragma unroll
      for (int g = 0; g < 4; ++g) {
        const f32x4 v = *(LAS f32x4*)(LB + L_SCR + (unsigned)(wave ^ 1) * 4096u + g * 1024 + lane * 16);
        hres[2 * g] = pk2(accA[4 * g] + v[0] + blo(xres[g][0]), accA[4 * g + 1] + v[1] + bhi(xres[g][0]));
        hres[2 * g + 1] = pk2(accA[4 * g + 2] + v[2] + blo(xres[g][1]), accA[4 * g + 3] + v[3] + bhi(xres[g][1]));
      }
      const unsigned o = OFF_H + (unsigned)(tt * 32 + 4 * c + 2 * kh) * 1024u + lane * 16;
      u32x4 h0 = {hres[0], hres[1], hres[2], hres[3]}, h1 = {hres[4], hres[5], hres[6], hres[7]};
      __builtin_amdgcn_raw_buffer_store_b128(h0, rWS, o, 0, 16);
      __builtin_amdgcn_raw_buffer_store_b128(h1, rWS, o + 1024, 0, 16);
      float sum = 0.f;
#pragma unroll
      for (int i = 0; i < 8; ++i) { hv[2 * i] = blo(hres[i]); hv[2 * i + 1] = bhi(hres[i]); sum += hv[2 * i] + hv[2 * i + 1]; }
      const float mean = swap32_add(sum) * (1.f / 32.f);
      float m2 = 0.f;
#pragma unroll
      for (int i = 0; i < 16; ++i) { const float d = hv[i] - mean; m2 = fmaf(d, d, m2); }
      m2 = swap32_add(m2);
      u32x2 st = {__float_as_uint(mean), __float_as_uint(m2)};
      if (h == 0) __builtin_amdgcn_raw_buffer_store_b64(st, rWS, OFF_HS + (unsigned)((tt * 16 + 2 * c + kh) * 256 + r * 8), 0, 16);
    }
    STAMP(19);
    VMWAIT(0);
    RBAR();
    (void)arrive(cntT + 32, lane);
    STAMP(10);

    const int t4 = 4 * G + wave;
    GAS unsigned* cnt4 = (GAS unsigned*)(ws + OFF_CNT + (unsigned)t4 * 1024u);
    wait_cnt(cnt4 + 32, 0u, 16u);
    STAMP(11);
    const unsigned hb = OFF_H + (unsigned)t4 * 32768u + lane * 16;
    u32x4 hf[32];
#pragma unroll
    for (int s = 0; s < 12; ++s) hf[s] = __builtin_amdgcn_raw_buffer_load_b128(rWS, hb + s * 1024, 0, 16);
    u32x2 stp[8];
    const unsigned w3e = LB + (unsigned)(trcb * 24576 + (4 * h + trq) * 192 + trp * 8 + trcb * 32);
    const unsigned w3o = LB + (unsigned)(trcb * 24576 + (4 * h + trq) * 192 + trp * 8 - trcb * 32);
    unsigned w3e1 = w3e + 49152u, w3o1 = w3o + 49152u, w3e2 = w3e + 98304u, w3o2 = w3o + 98304u, csa = LB + L_SCR + (unsigned)r * 16u;
    OPQ(w3e1); OPQ(w3o1); OPQ(w3e2); OPQ(w3o2); OPQ(csa);
    f32x16 ac[6];
#pragma unroll
    for (int u = 0; u < 6; ++u)
#pragma unroll
      for (int i = 0; i < 16; ++i) ac[u][i] = 0.f;
    float rsg, irs;
    u32x4 mfr;
    float tg[8];
    const unsigned hho = OFF_HH + (unsigned)(t4 * 86 + 2 * du0) * 1024u + lane * 16;
    const unsigned hho2 = ndu == 3 ? hho + 4096u : 0x7F000000u;
    auto epi = [&](int u, int k) {
      if (k == 1) {
#pragma unroll
        for (int i = 0; i < 8; ++i) tg[i] = ac[u][i] * rsg;
      } else if (k == 2 || k == 3) {
#pragma unroll
        for (int i = 4 * (k - 2); i < 4 * (k - 1); ++i) tg[i] = __builtin_amdgcn_exp2f(tg[i]);
      } else if (k == 4) {
#pragma unroll
        for (int i = 0; i < 8; ++i) tg[i] = fmaf(tg[i], irs, irs);
      } else if (k == 5 || k == 6) {
#pragma unroll
        for (int i = 4 * (k - 5); i < 4 * (k - 4); ++i) tg[i] = __builtin_amdgcn_rcpf(tg[i]);
      } else if (k == 7) {
#pragma unroll
        for (int i = 0; i < 8; ++i) tg[i] *= ac[u][8 + i];
      } else if (k == 8) {
        u32x4 o = {pk2(tg[0], tg[1]), pk2(tg[2], tg[3]), pk2(tg[4], tg[5]), pk2(tg[6], tg[7])};
        __builtin_amdgcn_raw_buffer_store_b128(o, rWS, u < 4 ? hho + u * 1024 : hho2 + (u - 4) * 1024, 0, 16);
      }
      if (k >= 1 && k <= 7) {
#pragma unroll
        for (int i = 0; i < 8; ++i) OPQ(tg[i]);
      }
    };
    pipe<198, 6>(
        [&](auto I_) __attribute__((always_inline)) {
          constexpr int i = I_.value;
          if constexpr (i < 144) {
            constexpr int u = i % 6, s = (i % 48) / 6;
            const unsigned a = ((i / 48 == 1) ? ((u & 1) ? w3o1 : w3e1) : ((u & 1) ? w3o : w3e)) + s * 3072 + u * 32;
            return trfrag(a, a + 1536);
          } else {
            constexpr int u = (i - 144) / 9, s = (i - 144) % 9;
            if constexpr (s == 8) return ldsfrag(csa + u * 512);
            else { const unsigned a = ((u & 1) ? w3o2 : w3e2) + s * 3072 + u * 32; return trfrag(a, a + 1536); }
          }
        },
        [&](auto I_, const bf16x8& w) __attribute__((always_inline)) {
          constexpr int i = I_.value;
          if constexpr (i < 144) {
            constexpr int u = i % 6, ks = i / 6, s = ks < 16 ? ks : ks + 8;
            MFMAP(ac[u], w, asfrag(hf[s]));
            if constexpr (u == 5 && ks + 12 < 32) { constexpr int k2 = ks + 12, s2 = k2 < 16 ? k2 : k2 < 24 ? k2 + 8 : k2 - 8; hf[s2] = __builtin_amdgcn_raw_buffer_load_b128(rWS, hb + s2 * 1024, 0, 16); }
            if (i == 121) {
#pragma unroll
              for (int q = 0; q < 8; ++q) stp[q] = __builtin_amdgcn_raw_buffer_load_b64(rWS, OFF_HS + (unsigned)((t4 * 16 + 2 * q + h) * 256 + r * 8), 0, 16);
            }
          } else {
            constexpr int u = (i - 144) / 9, s = (i - 144) % 9;
            if constexpr (s == 8) MFMAP(ac[u], w, asfrag(mfr));
            else MFMAP(ac[u], w, asfrag(hf[16 + s]));
            if (i == 146) {
              float ms = 0.f, q = 0.f;
#pragma unroll
              for (int k = 0; k < 8; ++k) { ms += __uint_as_float(stp[k][0]); q += __uint_as_float(stp[k][1]); }
              const float mean = swap32_add(ms) * (1.f / 16.f);
#pragma unroll
              for (int k = 0; k < 8; ++k) { const float d = __uint_as_float(stp[k][0]) - mean; q = fmaf(32.f * d, d, q); }
              q = swap32_add(q) * (1.f / 512.f) + 1e-5f;
              const float rs = __builtin_amdgcn_rsqf(q);
              rsg = rs * -1.44269504088896f;
              irs = q * rs;
              const unsigned a1 = pk2(-mean, 0.f) & 0xFFFFu;
              const float r1 = -mean - blo(a1);
              const unsigned a2 = pk2(r1, 0.f) & 0xFFFFu;
              const unsigned a3 = pk2(r1 - blo(a2), 0.f) & 0xFFFFu;
              mfr[0] = h ? 0u : (a1 | (a2 << 16)); mfr[1] = h ? 0u : (a1 | (a3 << 16)); mfr[2] = h ? 0u : (a2 | (a1 << 16)); mfr[3] = 0u;
            }
            if constexpr (u > 0) epi(u - 1, s);
            if (i == 144 + 43) { VMWAIT(1); signal(cnt4 + 64, lane); }
          }
        },
        [&](auto I_) __attribute__((always_inline)) {
          constexpr int i = I_.value;
          if (i == 54) RBAR();
          if (i == 96) { STAMP(21); RBAR(); STAMP(22); }
          if (i == 150) { RBAR(); STAMP(23); }
        });
    STAMP(12);
#pragma unroll
    for (int k = 1; k < 9; ++k) epi(5, k);
    VMWAIT(2);
    signal(cnt4 + 96, lane);
    VMWAIT(1);
    if (ndu == 3) signal(cnt4 + 128, lane);
    RBAR();
    STAMP(13);

    const unsigned gb = OFF_HH + (unsigned)(tt * 86 + kh) * 1024u + lane * 16;
    u32x4 gf[43];
    wait_cnt(cntT + 64, 0u, 16u);
    STAMP(14);
#pragma unroll
    for (int m = 0; m < 16; ++m) gf[m] = __builtin_amdgcn_raw_buffer_load_b128(rWS, gb + Dm(m) * 2048, 0, 16);
    STAMP(24);
    RBAR();
    STAMP(25);
#pragma unroll
    for (int m = 16; m < 36; ++m) gf[m] = __builtin_amdgcn_raw_buffer_load_b128(rWS, gb + Dm(m) * 2048, 0, 16);
    const unsigned dA = LB + (unsigned)kh * 2048u + trpm + (unsigned)((kh ^ (trq >> 1)) * 64);
    const unsigned dB = dA ^ 64u;
    unsigned dA1 = dA + 49152u, dB1 = dB + 49152u, dA2 = dA + 98304u, dB2 = dB + 98304u;
    OPQ(dA1); OPQ(dB1); OPQ(dA2); OPQ(dB2);
#pragma unroll
    for (int i = 0; i < 16; ++i) { accA[i] = 0.f; accB[i] = 0.f; }
    pipe<86, 6>(
        [&](auto I_) __attribute__((always_inline)) {
          constexpr int i = I_.value;
          constexpr int m = i >> 1, p = m / 12, mm = m % 12;
          const unsigned a = ((i & 1) ? ((p == 0 || p == 3) ? dA1 : p == 1 ? dA : dA2) : ((p == 0 || p == 3) ? dB1 : p == 1 ? dB : dB2)) + mm * 4096;
          return trfrag(a, a + 1024);
        },
        [&](auto I_, const bf16x8& w) __attribute__((always_inline)) {
          constexpr int i = I_.value;
          if (i & 1) MFMAP(accA, w, asfrag(gf[i >> 1])); else MFMAP(accB, w, asfrag(gf[i >> 1]));
          if constexpr ((i & 1) && (i >> 1) < 7) gf[36 + (i >> 1)] = __builtin_amdgcn_raw_buffer_load_b128(rWS, gb + Dm(36 + (i >> 1)) * 2048, 0, 16);
        },
        [&](auto I_) __attribute__((always_inline)) {
          constexpr int i = I_.value;
          if (i == 30) RBAR();
          if (i == 48) RBAR();
          if (i == 72) RBAR();
        });
    STAMP(26);
    RBAR();
    {
      const unsigned ob = LB + (unsigned)kh * 17408u + (unsigned)(32 * tau + r) * 272u + (unsigned)h * 16u;
#pragma unroll
      for (int g = 0; g < 4; ++g) {
        const f32x4 va = {accA[4 * g] + blo(hres[2 * g]), accA[4 * g + 1] + bhi(hres[2 * g]), accA[4 * g + 2] + blo(hres[2 * g + 1]), accA[4 * g + 3] + bhi(hres[2 * g + 1])};
        const f32x4 vb = {accB[4 * g], accB[4 * g + 1], accB[4 * g + 2], accB[4 * g + 3]};
        *(LAS f32x4*)(ob + (unsigned)kh * 128u + g * 32) = va;
        *(LAS f32x4*)(ob + (unsigned)(kh ^ 1) * 128u + g * 32) = vb;
      }
    }
    BAR();
    STAMP(15);
  } else {
    const int hw = wave - 4;
    auto w3blk = [&](int blk, unsigned dst) {
#pragma unroll 1
      for (int p = 0; p < 2; ++p) {
        const unsigned row0 = (unsigned)(32 * hw + 16 * p), so = (128u * blk + row0) * 2752u + 64u * du0, ld = LB + dst + row0 * 192u;
#pragma unroll
        for (int i = 0; i < 3; ++i) {
          dma16(rWG, v3g[i], so, ld + i * 1024u);
          dma16(rWU, v3u[i], so, ld + 24576u + i * 1024u);
        }
      }
    };
    VMWAIT(0);
    signal(cntT, lane);
    RBAR();
    w3blk(1, 49152);
    u32x4 b3[12];
#pragma unroll
    for (int q = 0; q < 12; ++q) {
      const unsigned so = (384u + (unsigned)(32 * hw + 16 * (q / 6))) * 2752u + 64u * du0;
      if ((q % 6) < 3) asm volatile("buffer_load_dwordx4 %0, %1, %2, %3 offen" : "=v"(b3[q]) : "v"(v3g[q % 3]), "s"(rWG), "s"(so) : "memory");
      else asm volatile("buffer_load_dwordx4 %0, %1, %2, %3 offen" : "=v"(b3[q]) : "v"(v3u[q % 3]), "s"(rWU), "s"(so) : "memory");
    }
    VMWAIT(12);
    RBAR();
    RBAR();
    RBAR();
    w3blk(2, 98304);
    asm volatile("s_waitcnt vmcnt(0)" : "+v"(b3[0]), "+v"(b3[1]), "+v"(b3[2]), "+v"(b3[3]), "+v"(b3[4]), "+v"(b3[5]), "+v"(b3[6]), "+v"(b3[7]), "+v"(b3[8]), "+v"(b3[9]), "+v"(b3[10]), "+v"(b3[11])::"memory");
    if (hw == (G & 3)) {
      const int blk = G >> 2;
      if (blk == 3) {
#pragma unroll
        for (int q = 0; q < 12; ++q) ldsst(LB + L_SCR + (unsigned)(16 * (q / 6)) * 192u + ((q % 6) >= 3 ? 6144u : 0u) + (unsigned)(q % 3) * 1024u + lane * 16, b3[q]);
        asm volatile("s_waitcnt lgkmcnt(0)" ::: "memory");
      }
      const unsigned we = LB + (blk == 3 ? L_SCR + (unsigned)trcb * 6144u : (unsigned)(blk * 49152 + trcb * 24576 + 32 * hw * 192)) + (unsigned)((4 * h + trq) * 192 + trp * 8 + trcb * 32);
      const unsigned wo = we - (unsigned)(trcb * 64);
      f32x16 cs;
#pragma unroll
      for (int i = 0; i < 16; ++i) cs[i] = 0.f;
      bf16x8 f[12];
#pragma unroll
      for (int q = 0; q < 12; ++q) { const int u = q >> 1; const unsigned a = ((u & 1) ? wo : we) + (q & 1) * 3072 + u * 32; f[q] = trfrag(a, a + 1536); }
#pragma unroll
      for (int q = 0; q < 12; ++q) {
        const unsigned sel = (r == (q >> 1)) ? 0x3F803F80u : 0u;
        const u32x4 sv = {sel, sel, sel, sel};
        cs = MFMA(f[q], asfrag(sv), cs);
      }
      if (r < 6) {
        const unsigned tg32 = (unsigned)T;
#pragma unroll
        for (int g = 0; g < 16; ++g)
          __hip_atomic_store((GAS u64*)(ws + OFF_CS2 + (unsigned)((j * 16 + G) * 1536 + (r * 32 + (g >> 2) * 8 + h * 4 + (g & 3)) * 8)), ((u64)tg32 << 32) | __float_as_uint(cs[g]), RLX_AGENT);
      }
    }
    if (b == 0 && hw == 3) {
      unsigned spins = 0;
#pragma unroll 1
      for (;;) {
        bool ok = true;
#pragma unroll
        for (int i = 0; i < 4; ++i) ok &= __hip_atomic_load((const GAS u64*)(ws + OFF_KF + (unsigned)(i * 64 + lane) * 128u), RLX_AGENT) == T;
        if (__all(ok)) break;
        __builtin_amdgcn_s_sleep(2);
        if (++spins > SPIN_CAP) break;
      }
      if (lane == 0) __hip_atomic_store((GAS u64*)(ws + OFF_EPOCH), T, RLX_AGENT);
    }
    STAMP(20);
    RBAR();
#pragma unroll
    for (int q = 0; q < 12; ++q)
      ldsst(LB + (unsigned)(32 * hw + 16 * (q / 6)) * 192u + ((q % 6) >= 3 ? 24576u : 0u) + (unsigned)(q % 3) * 1024u + lane * 16, b3[q]);
    STAMP(21);
    BAR();
    if (hw < 3) {
      float pv[16];
      unsigned spins = 0;
#pragma unroll 1
      for (;;) {
        bool ok = true;
#pragma unroll
        for (int g = 0; g < 16; ++g) {
          const u64 x = __hip_atomic_load((const GAS u64*)(ws + OFF_CS2 + (unsigned)((j * 16 + g) * 1536 + (hw * 64 + lane) * 8)), RLX_AGENT);
          pv[g] = __uint_as_float((unsigned)x);
          ok &= (unsigned)(x >> 32) == (unsigned)T;
        }
        if (__all(ok)) break;
        __builtin_amdgcn_s_sleep(2);
        if (++spins > SPIN_CAP) break;
      }
      const float v = (((pv[0] + pv[1]) + (pv[2] + pv[3])) + ((pv[4] + pv[5]) + (pv[6] + pv[7]))) + (((pv[8] + pv[9]) + (pv[10] + pv[11])) + ((pv[12] + pv[13]) + (pv[14] + pv[15])));
      const unsigned c1 = pk2(v, 0.f) & 0xFFFFu;
      const float r1 = v - blo(c1);
      const unsigned c2 = pk2(r1, 0.f) & 0xFFFFu;
      const unsigned c3 = pk2(r1 - blo(c2), 0.f) & 0xFFFFu;
      const u32x4 f = {c1 | (c1 << 16), c2 | (c1 << 16), c2 | (c3 << 16), 0u};
      ldsst(LB + L_SCR + (unsigned)(hw * 64 + lane) * 16u, f);
    }
    BAR();
    auto w4 = [&](int m0, int n, unsigned dst) {
#pragma unroll 1
      for (int q = 0; q < n; ++q) {
        const int mm = hw + 4 * q;
        dma_run4(rWD, v1k, (unsigned)Dm(m0 + mm) * 32768u + 128u * c, 8192, LB + dst + (unsigned)mm * 4096u, 1);
      }
    };
    w4(0, 3, 49152);
    w4(12, 3, 0);
    RBAR();
    w4(24, 3, 98304);
    wait_cnt(cntT + 96 + 32 * hp, 0u, hp ? 11u : 16u);
    VMWAIT(0);
    RBAR();
    RBAR();
    w4(36, 2, 49152);
    VMWAIT(8);
    RBAR();
    VMWAIT(0);
    RBAR();
    RBAR();
    RBAR();
  }
  {
    const int row = threadIdx.x >> 3, ch = threadIdx.x & 7;
    const unsigned a = LB + (unsigned)row * 272u + (unsigned)ch * 32u;
    const f32x4 p0 = *(LAS f32x4*)a + *(LAS f32x4*)(a + 17408), p1 = *(LAS f32x4*)(a + 16) + *(LAS f32x4*)(a + 17408 + 16);
    const u32x4 o = {pk2(p0[0], p0[1]), pk2(p0[2], p0[3]), pk2(p1[0], p1[1]), pk2(p1[2], p1[3])};
    __builtin_amdgcn_raw_buffer_store_b128(o, rOUT, (unsigned)((64 * H + row) * 1024 + 128 * c + ch * 16), 0, 16);
  }
  STAMP_END();
}

extern "C" void kernel_launch(void* const* d_in, const int* in_sizes, int n_in, void* d_out, int out_size, void* d_ws, size_t ws_size,
                              hipStream_t stream) {
  (void)in_sizes; (void)n_in; (void)out_size; (void)ws_size;
  (void)hipFuncSetAttribute((const void*)fused_block, hipFuncAttributeMaxDynamicSharedMemorySize, (int)LDS_BYTES);
  hipLaunchKernelGGL(fused_block, dim3(256), dim3(512), LDS_BYTES, stream, d_in[0], d_in[1], d_in[2], d_in[4], d_in[5], d_in[6], d_out, (char*)d_ws);
}
```

```cpp
#include <hip/hip_runtime.h>
#include <hip/hip_bf16.h>
#include <utility>

typedef __bf16 bf16x8 __attribute__((ext_vector_type(8)));
typedef __bf16 bf16x4 __attribute__((ext_vector_type(4)));
typedef __bf16 bf16x2 __attribute__((ext_vector_type(2)));
typedef float f32x16 __attribute__((ext_vector_type(16)));
typedef float f32x4 __attribute__((ext_vector_type(4)));
typedef float f32x2 __attribute__((ext_vector_type(2)));
typedef unsigned u32x4 __attribute__((ext_vector_type(4)));
typedef unsigned u32x2 __attribute__((ext_vector_type(2)));
typedef unsigned long long u64;
#define LAS __attribute__((address_space(3)))
#define GAS __attribute__((address_space(1)))
#define DI __device__ __forceinline__
#define RLX_AGENT __ATOMIC_RELAXED, __HIP_MEMORY_SCOPE_AGENT
#define MFMA(a, b, c) __builtin_amdgcn_mfma_f32_32x32x16_bf16((a), (b), (c), 0, 0, 0)
#define MFMAP(acc, a, b) do { acc = MFMA(a, b, acc); asm volatile("" : "+v"(acc)); } while (0)
#define OPQ(x) asm volatile("" : "+v"(x))
typedef __amdgpu_buffer_rsrc_t rsrc_t;

constexpr unsigned OFF_EPOCH = 0;
constexpr unsigned OFF_READY = 4096;
constexpr unsigned OFF_KF = 16384;
constexpr unsigned OFF_VF = OFF_KF + 32768;
constexpr unsigned OFF_CNT = OFF_VF + 32768;
constexpr unsigned OFF_CS2 = 9u * (1u << 20) + (512u << 10);
constexpr unsigned OFF_STAMP = 512u << 10;
constexpr unsigned OFF_K = 1u << 20;
constexpr unsigned OFF_V = 3u << 20;
constexpr unsigned OFF_O = 5u << 20;
constexpr unsigned OFF_H = 7u << 20;
constexpr unsigned OFF_HS = 9u << 20;
constexpr unsigned OFF_HH = 10u << 20;
constexpr unsigned WS_BYTES = 16u << 20;
constexpr unsigned KB = 1024;
constexpr unsigned L_WK = 0, L_WV = 64 * KB, L_XLN = 96 * KB, L_WQA = 128 * KB, L_WQB = 32 * KB;
constexpr unsigned L_K = 0, L_V = 48 * KB, L_QH = 40 * KB, L_ONES = 88 * KB, L_WO = 96 * KB;
constexpr unsigned L_SCR = 144 * KB;
constexpr unsigned LDS_BYTES = 160 * KB;
constexpr unsigned SPIN_CAP = 1u << 18;

#define BAR() do { asm volatile("s_waitcnt lgkmcnt(0)" ::: "memory"); __builtin_amdgcn_s_barrier(); asm volatile("" ::: "memory"); } while (0)
#define VMWAIT(n) asm volatile("s_waitcnt vmcnt(" #n ")" ::: "memory")

#define STAMP(i) do {} while (0)
#define STAMP_END() do {} while (0)

DI unsigned rfl(unsigned x) { return (unsigned)__builtin_amdgcn_readfirstlane((int)x); }
DI void dma16(rsrc_t rs, unsigned voff, unsigned soff, unsigned lds) {
  asm volatile("s_mov_b32 m0, %0\n\ts_nop 0\n\tbuffer_load_dwordx4 %1, %2, %3 offen lds" ::"s"(lds), "v"(voff), "s"(rs), "s"(soff) : "m0", "memory");
}
DI void dma16_sc1(rsrc_t rs, unsigned voff, unsigned soff, unsigned lds) {
  asm volatile("s_mov_b32 m0, %0\n\ts_nop 0\n\tbuffer_load_dwordx4 %1, %2, %3 offen sc1 lds" ::"s"(lds), "v"(voff), "s"(rs), "s"(soff) : "m0", "memory");
}
DI void dma_run4(rsrc_t rs, unsigned voff, unsigned soff, unsigned step, unsigned lds, int n4) {
#pragma unroll 1
  for (int i = 0; i < n4; ++i) {
    asm volatile(
        "s_mov_b32 m0, %[l]\n\ts_nop 0\n\tbuffer_load_dwordx4 %[v], %[r], %[s] offen lds\n\t"
        "s_add_u32 m0, m0, 0x400\n\ts_add_u32 %[s], %[s], %[st]\n\tbuffer_load_dwordx4 %[v], %[r], %[s] offen lds\n\t"
        "s_add_u32 m0, m0, 0x400\n\ts_add_u32 %[s], %[s], %[st]\n\tbuffer_load_dwordx4 %[v], %[r], %[s] offen lds\n\t"
        "s_add_u32 m0, m0, 0x400\n\ts_add_u32 %[s], %[s], %[st]\n\tbuffer_load_dwordx4 %[v], %[r], %[s] offen lds\n\t"
        "s_add_u32 %[s], %[s], %[st]\n\ts_add_u32 %[l], %[l], 0x1000"
        : [s] "+s"(soff), [l] "+s"(lds) : [v] "v"(voff), [r] "s"(rs), [st] "s"(step) : "m0", "scc", "memory");
  }
}
DI void dma_run4_sc1(rsrc_t rs, unsigned voff, unsigned soff, unsigned lds) {
  asm volatile(
      "s_mov_b32 m0, %[l]\n\ts_nop 0\n\tbuffer_load_dwordx4 %[v], %[r], %[s] offen sc1 lds\n\t"
      "buffer_load_dwordx4 %[v], %[r], %[s] offen offset:1024 sc1 lds\n\t"
      "buffer_load_dwordx4 %[v], %[r], %[s] offen offset:2048 sc1 lds\n\t"
      "buffer_load_dwordx4 %[v], %[r], %[s] offen offset:3072 sc1 lds"
      ::[s] "s"(soff), [l] "s"(lds), [v] "v"(voff), [r] "s"(rs) : "m0", "memory");
}
#define RBAR() do { asm volatile("" ::: "memory"); __builtin_amdgcn_s_barrier(); asm volatile("" ::: "memory"); } while (0)
#define SCHED() __builtin_amdgcn_sched_barrier(0)
template <int... I, class F> DI void sfor_(std::integer_sequence<int, I...>, F&& f) { (f(std::integral_constant<int, I>{}), ...); }
template <int N, class F> DI void sfor(F&& f) { sfor_(std::make_integer_sequence<int, N>{}, f); }
template <int N, int D, class LD, class MM, class PRE>
DI void pipe(LD&& ld, MM&& mm, PRE&& pre) {
  bf16x8 buf[D];
  sfor<D>([&](auto I) { pre(I); buf[I.value] = ld(I); });
  SCHED();
  sfor<N>([&](auto S) {
    constexpr int s = S.value;
    mm(S, buf[s % D]);
    if constexpr (s + D < N) { pre(std::integral_constant<int, s + D>{}); buf[s % D] = ld(std::integral_constant<int, s + D>{}); }
    SCHED();
  });
}
DI bf16x8 trfrag(unsigned a0, unsigned a1) {
  bf16x4 lo = __builtin_amdgcn_ds_read_tr16_b64_v4bf16((LAS bf16x4*)a0);
  bf16x4 hi = __builtin_amdgcn_ds_read_tr16_b64_v4bf16((LAS bf16x4*)a1);
  return __builtin_shufflevector(lo, hi, 0, 1, 2, 3, 4, 5, 6, 7);
}
DI bf16x8 ldsfrag(unsigned a) { return *(LAS bf16x8*)a; }
DI void ldsst(unsigned a, u32x4 v) { *(LAS u32x4*)a = v; }
DI unsigned pk2(float a, float b) { f32x2 v = {a, b}; return __builtin_bit_cast(unsigned, __builtin_convertvector(v, bf16x2)); }
DI u32x4 pack8(const f32x16& x, int o) { u32x4 r; r[0] = pk2(x[o], x[o + 1]); r[1] = pk2(x[o + 2], x[o + 3]); r[2] = pk2(x[o + 4], x[o + 5]); r[3] = pk2(x[o + 6], x[o + 7]); return r; }
DI float blo(unsigned u) { return __uint_as_float(u << 16); }
DI float bhi(unsigned u) { return __uint_as_float(u & 0xffff0000u); }
DI bf16x8 asfrag(u32x4 v) { return __builtin_bit_cast(bf16x8, v); }
DI float swap32_add(float a) {
  auto r = __builtin_amdgcn_permlane32_swap(__float_as_uint(a), __float_as_uint(a), false, false);
  return __uint_as_float(r[0]) + __uint_as_float(r[1]);
}
DI float swap32_max(float a) {
  auto r = __builtin_amdgcn_permlane32_swap(__float_as_uint(a), __float_as_uint(a), false, false);
  return fmaxf(__uint_as_float(r[0]), __uint_as_float(r[1]));
}
template <int CTRL> DI float dpp_add(float a) { return a + __uint_as_float(__builtin_amdgcn_update_dpp(0, __float_as_uint(a), CTRL, 0xf, 0xf, true)); }
DI float row16_sum(float a) { a = dpp_add<0x128>(a); a = dpp_add<0x124>(a); a = dpp_add<0x122>(a); a = dpp_add<0x121>(a); return a; }
DI void red8(const float (&v)[8], float& A, float& B) {
  float t[4];
#pragma unroll
  for (int i = 0; i < 4; ++i) {
    auto r = __builtin_amdgcn_permlane32_swap(__float_as_uint(v[2 * i]), __float_as_uint(v[2 * i + 1]), false, false);
    t[i] = __uint_as_float(r[0]) + __uint_as_float(r[1]);
  }
  auto r0 = __builtin_amdgcn_permlane16_swap(__float_as_uint(t[0]), __float_as_uint(t[1]), false, false);
  auto r1 = __builtin_amdgcn_permlane16_swap(__float_as_uint(t[2]), __float_as_uint(t[3]), false, false);
  A = row16_sum(__uint_as_float(r0[0]) + __uint_as_float(r0[1]));
  B = row16_sum(__uint_as_float(r1[0]) + __uint_as_float(r1[1]));
}
DI float rdl(float a, int l) { return __uint_as_float((unsigned)__builtin_amdgcn_readlane((int)__float_as_uint(a), l)); }
DI void bc8(float A, float B, float (&o)[8]) {
  o[0] = rdl(A, 0); o[2] = rdl(A, 16); o[1] = rdl(A, 32); o[3] = rdl(A, 48);
  o[4] = rdl(B, 0); o[6] = rdl(B, 16); o[5] = rdl(B, 32); o[7] = rdl(B, 48);
}
DI void wait_cnt(GAS unsigned* c, unsigned have, unsigned target) {
  unsigned spins = 0;
#pragma unroll 1
  while (have < target) {
    __builtin_amdgcn_s_sleep(4);
    have = rfl(__hip_atomic_load(c, RLX_AGENT));
    if (++spins > SPIN_CAP) break;
  }
  __builtin_amdgcn_fence(__ATOMIC_ACQUIRE, "wavefront");
}
DI unsigned arrive(GAS unsigned* c, int lane) {
  unsigned r = 0;
  if (lane == 0) r = __hip_atomic_fetch_add(c, 1u, RLX_AGENT) + 1u;
  r = rfl(r);
  asm volatile("" ::"s"(r));
  return r;
}

__host__ __device__ constexpr int Dm(int m) { return m < 32 ? ((43 * (m & 15)) >> 4) + (m >> 4) : (4 + 8 * ((m - 32) >> 1) + 3 * ((m - 32) & 1) < 42 ? 4 + 8 * ((m - 32) >> 1) + 3 * ((m - 32) & 1) : 42); }

template <bool FULL, class MID, class HOOK, class ST>
DI u32x4 attention(const unsigned ka, const unsigned va, const bf16x8 q, const int n0, const int e, const int r, const int h, const bool helper, MID&& mid, HOOK&& hook, ST&& stamp) {
  bf16x8 kf[9];
#pragma unroll
  for (int n = 0; n < 9; ++n) kf[n] = ldsfrag(ka + n * 4096);
  f32x16 zero;
#pragma unroll
  for (int i = 0; i < 16; ++i) zero[i] = 0.f;
  f32x16 sm = MFMA(kf[8], q, zero);
  f32x16 sa, sb;
  if (FULL) {
    sb = MFMA(kf[0], q, zero);
    sa = MFMA(kf[1], q, zero);
    OPQ(sa);
  } else {
#pragma unroll
    for (int i = 0; i < 16; ++i) sb[i] = -1e30f;
  }
#pragma unroll
  for (int i = 0; i < 16; ++i) { const int kl = (i & 3) + 8 * (i >> 2) + 4 * h; sm[i] = (kl <= r) ? sm[i] : sb[i]; }
  float mx = sm[0], mx2 = sm[1];
#pragma unroll
  for (int i = 2; i < 16; i += 2) mx = fmaxf(fmaxf(mx, sm[i]), sm[i + 1]);
  if (FULL) {
    SCHED();
#pragma unroll
    for (int n = 1; n < 8; ++n) {
      if (n & 1) {
        if (n < 7) { sb = MFMA(kf[n + 1], q, zero); OPQ(sb); }
        SCHED();
#pragma unroll
        for (int i = 0; i < 16; i += 4) { mx = fmaxf(fmaxf(mx, sa[i]), sa[i + 1]); mx2 = fmaxf(fmaxf(mx2, sa[i + 2]), sa[i + 3]); }
      } else {
        sa = MFMA(kf[n + 1], q, zero); OPQ(sa);
        SCHED();
#pragma unroll
        for (int i = 0; i < 16; i += 4) { mx = fmaxf(fmaxf(mx, sb[i]), sb[i + 1]); mx2 = fmaxf(fmaxf(mx2, sb[i + 2]), sb[i + 3]); }
      }
      SCHED();
      if (n == 3) mid();
      if (n >= 4) hook(4 + n);
    }
  } else {
    mid();
#pragma unroll
    for (int n = 8; n < 12; ++n) hook(n);
#pragma unroll
    for (int n = 1; n < 8; ++n) {
      if (n >= n0) {
        const f32x16 s = MFMA(kf[n], q, zero);
#pragma unroll
        for (int i = 0; i < 16; ++i) mx = fmaxf(mx, s[i]);
      }
    }
  }
  mx = swap32_max(fmaxf(mx, mx2));
  f32x16 negm;
#pragma unroll
  for (int i = 0; i < 16; ++i) negm[i] = -mx;
  if (FULL) { sa = MFMA(kf[1], q, negm); OPQ(sa); }
  u32x4 p0, p1, d0, d1;
  {
    f32x16 p;
#pragma unroll
    for (int i = 0; i < 16; ++i) p[i] = __builtin_amdgcn_exp2f(sm[i] - mx);
    p0 = pack8(p, 0); p1 = pack8(p, 8);
#pragma unroll
    for (int k = 0; k < 4; ++k) {
      const int i0 = 2 * k, i1 = 8 + 2 * k;
      const int ka0 = (i0 & 3) + 8 * (i0 >> 2) + 4 * h, ka1 = (i1 & 3) + 8 * (i1 >> 2) + 4 * h;
      const unsigned m0 = (ka0 <= r ? 0xFFFFu : 0u) | (ka0 + 1 <= r ? 0xFFFF0000u : 0u);
      const unsigned m1 = (ka1 <= r ? 0xFFFFu : 0u) | (ka1 + 1 <= r ? 0xFFFF0000u : 0u);
      d0[k] = p0[k] & m0; d1[k] = p1[k] & m1;
    }
    p0 ^= d0; p1 ^= d1;
  }
  OPQ(p0); OPQ(p1); OPQ(d0); OPQ(d1);
  stamp(16);
  if (helper) VMWAIT(8);
  RBAR();
  stamp(7);
  const bool ones = ((r >> 4) != e);
  unsigned vp = ones ? L_ONES : va;
  const unsigned i1k = ones ? 0u : 1024u, i3k = ones ? 0u : 3072u;
  f32x16 o = zero;
  if (FULL) {
    const unsigned v8 = vp + 32 * i1k;
    const bf16x8 w80 = ldsfrag(v8), w81 = ldsfrag(v8 + i1k);
    const bf16x8 w00 = ldsfrag(vp); vp += i1k;
    const bf16x8 w01 = ldsfrag(vp); vp += i3k;
    bf16x8 v0 = ldsfrag(vp); vp += i1k;
    bf16x8 v1 = ldsfrag(vp); vp += i3k;
    o = MFMA(w80, asfrag(d0), o);
    o = MFMA(w81, asfrag(d1), o);
    o = MFMA(w00, asfrag(p0), o);
    o = MFMA(w01, asfrag(p1), o);
    OPQ(o);
    SCHED();
#pragma unroll
    for (int n = 1; n < 8; ++n) {
      bf16x8 n0f, n1f;
      if (n < 7) {
        if (n & 1) { sb = MFMA(kf[n + 1], q, negm); OPQ(sb); } else { sa = MFMA(kf[n + 1], q, negm); OPQ(sa); }
        n0f = ldsfrag(vp); vp += i1k;
        n1f = ldsfrag(vp); vp += i3k;
        hook(n - 1);
      }
      SCHED();
      f32x16& s = (n & 1) ? sa : sb;
#pragma unroll
      for (int i = 0; i < 8; ++i) s[i] = __builtin_amdgcn_exp2f(s[i]);
      MFMAP(o, v0, asfrag(pack8(s, 0)));
      SCHED();
#pragma unroll
      for (int i = 8; i < 16; ++i) s[i] = __builtin_amdgcn_exp2f(s[i]);
      MFMAP(o, v1, asfrag(pack8(s, 8)));
      SCHED();
      v0 = n0f; v1 = n1f;
    }
  } else {
#pragma unroll
    for (int n = 0; n < 6; ++n) hook(n);
    o = MFMA(ldsfrag(vp + 32 * i1k), asfrag(d0), o);
    o = MFMA(ldsfrag(vp + 33 * i1k), asfrag(d1), o);
#pragma unroll
    for (int n = 1; n < 8; ++n) {
      if (n >= n0) {
        f32x16 p = MFMA(kf[n], q, negm);
#pragma unroll
        for (int i = 0; i < 16; ++i) p[i] = __builtin_amdgcn_exp2f(p[i]);
        o = MFMA(ldsfrag(vp + (4 * n) * i1k), asfrag(pack8(p, 0)), o);
        o = MFMA(ldsfrag(vp + (4 * n + 1) * i1k), asfrag(pack8(p, 8)), o);
      }
    }
  }
  u32x4 of;
  if (e == 0) {
    const float il = __builtin_amdgcn_rcpf(o[8]);
#pragma unroll
    for (int i = 0; i < 8; ++i) o[i] *= il;
    of = pack8(o, 0);
    OPQ(of);
  } else {
    const float il = __builtin_amdgcn_rcpf(o[0]);
#pragma unroll
    for (int i = 8; i < 16; ++i) o[i] *= il;
    of = pack8(o, 8);
    OPQ(of);
  }
  return of;
}

DI void ln8(const u32x4* xr, const unsigned wb, const int lane) {
  float s1[8], s2[8], rs[8], nm[8];
  const bf16x2 one2 = __builtin_bit_cast(bf16x2, 0x3F803F80u);
#pragma unroll
  for (int i = 0; i < 8; ++i) {
    float a = 0.f, q = 0.f;
#pragma unroll
    for (int k = 0; k < 4; ++k) {
      const unsigned xu = xr[i][k];
      const bf16x2 v = __builtin_bit_cast(bf16x2, xu);
      a = __builtin_amdgcn_fdot2_f32_bf16(v, one2, a, false);
      q = __builtin_amdgcn_fdot2_f32_bf16(v, v, q, false);
    }
    s1[i] = a; s2[i] = q;
  }
  float A1, B1, A2, B2;
  red8(s1, A1, B1);
  red8(s2, A2, B2);
  A1 *= (1.f / 512.f); B1 *= (1.f / 512.f);
  A2 = __builtin_amdgcn_rsqf(fmaxf(fmaf(-A1, A1, A2 * (1.f / 512.f)), 0.f) + 1e-5f);
  B2 = __builtin_amdgcn_rsqf(fmaxf(fmaf(-B1, B1, B2 * (1.f / 512.f)), 0.f) + 1e-5f);
  bc8(A2, B2, rs);
  bc8(-A1 * A2, -B1 * B2, nm);
#pragma unroll
  for (int i = 0; i < 8; ++i) {
    u32x4 o;
#pragma unroll
    for (int k = 0; k < 4; ++k) o[k] = pk2(fmaf(blo(xr[i][k]), rs[i], nm[i]), fmaf(bhi(xr[i][k]), rs[i], nm[i]));
    ldsst(wb + (unsigned)((i ^ (lane & 7)) << 4), o);
  }
}
DI void poll_tag(const GAS u64* p, const u64 T) {
  unsigned spins = 0;
#pragma unroll 1
  for (;;) {
    const u64 v = __hip_atomic_load(p, RLX_AGENT);
    if (__all(v == T)) break;
    __builtin_amdgcn_s_sleep(1);
    if (++spins > SPIN_CAP) break;
  }
  asm volatile("" ::: "memory");
}
DI void signal(GAS unsigned* c, int lane) { if (lane == 0) (void)__hip_atomic_fetch_add(c, 1u, RLX_AGENT); }

__global__ void __launch_bounds__(512, 2)
fused_block(const void* __restrict__ gx, const void* __restrict__ gwqkv, const void* __restrict__ gwo, const void* __restrict__ gwg,
            const void* __restrict__ gwu, const void* __restrict__ gwd, void* __restrict__ gout, char* ws) {
  extern __shared__ __attribute__((aligned(16))) char smem[];
  const int lane = threadIdx.x & 63;
  const int wave = (int)rfl(threadIdx.x >> 6);
  STAMP(0);
  const int b = blockIdx.x, xcd = b & 7, idx = b >> 3;
  const int G = xcd * 2 + (idx >> 4), j = idx & 15, t = j >> 3, c = j & 7, H = 2 * G + t;
  const int r = lane & 31, h = lane >> 5;
  constexpr unsigned LB = 0;
  asm volatile("" ::"v"(smem));
  const rsrc_t rX = __builtin_amdgcn_make_buffer_rsrc((void*)gx, 0, 2048 * 512 * 2, 0x00020000);
  const rsrc_t rWQKV = __builtin_amdgcn_make_buffer_rsrc((void*)gwqkv, 0, 512 * 1536 * 2, 0x00020000);
  const rsrc_t rWO = __builtin_amdgcn_make_buffer_rsrc((void*)gwo, 0, 512 * 512 * 2, 0x00020000);
  const rsrc_t rWG = __builtin_amdgcn_make_buffer_rsrc((void*)gwg, 0, 512 * 1376 * 2, 0x00020000);
  const rsrc_t rWU = __builtin_amdgcn_make_buffer_rsrc((void*)gwu, 0, 512 * 1376 * 2, 0x00020000);
  const rsrc_t rWD = __builtin_amdgcn_make_buffer_rsrc((void*)gwd, 0, 1376 * 512 * 2, 0x00020000);
  const rsrc_t rOUT = __builtin_amdgcn_make_buffer_rsrc(gout, 0, 2048 * 512 * 2, 0x00020000);
  const rsrc_t rWS = __builtin_amdgcn_make_buffer_rsrc((void*)ws, 0, WS_BYTES, 0x00020000);

  const unsigned chsw = (unsigned)(((lane & 7) ^ (((lane >> 4) & 1) << 2)) * 16);
  const unsigned v1k = (unsigned)(lane >> 3) * 1024u + chsw;
  const int trq = (lane & 15) >> 2, trp = lane & 3, trcb = (lane >> 4) & 1;
  const int tau = (wave & 3) >> 1, hp = wave & 1;
  const int tt = 2 * H + tau;
  GAS unsigned* cntT = (GAS unsigned*)(ws + OFF_CNT + (unsigned)tt * 1024u);
  u32x4 qf;
  u32x2 xres[4];
  u64 T;

  const unsigned vq = (unsigned)(lane >> 3) * 3072u + chsw;
  {
    asm volatile("s_load_dwordx2 %0, %1, 0x0" : "=s"(T) : "s"(ws));
    if (j == 0 && wave == 4 && lane < 32) __hip_atomic_store((GAS unsigned*)(ws + OFF_CNT + (unsigned)(32 * G + lane) * 128u), 0u, RLX_AGENT);
    {
      const unsigned m0v = L_WK + (unsigned)wave * 8192u;
      if (wave == 6) {
        u64 pc;
        asm volatile("s_getpc_b64 %0" : "=s"(pc));
        const u64 a = (pc & ~127ull) + (u64)((idx * 9 + (lane < 9 ? lane : 0)) * 128)   ;
        asm volatile("s_mov_b32 m0, %1\n\ts_nop 0\n\tglobal_load_lds_dword %0, off" ::"v"(a), "s"(m0v) : "m0", "memory");
      } else {
        const int n = wave == 0 ? 48 : wave == 5 ? 8 : 1;
        const int ln = lane < n ? lane : 0;
        const unsigned po = wave == 5 ? (unsigned)ln * (2u << 20) + (1u << 20) : wave == 7 ? (unsigned)(32 * tt) * 1024u : (unsigned)(b * n + ln) * 128u;
        const rsrc_t rp = wave == 0 ? rWQKV : wave == 1 ? rWG : wave == 2 ? rWU : wave == 3 ? rWD : wave == 4 ? rWO : wave == 5 ? rWS : rOUT;
        asm volatile("s_mov_b32 m0, %2\n\ts_nop 0\n\tbuffer_load_dword %0, %1, 0 offen lds" ::"v"(po), "s"(rp), "s"(m0v) : "m0", "memory");
      }
    }
#define PFN 1
    u32x4 xr[8];
    const unsigned vo = lane * 16, so = (unsigned)(64 * H + 8 * wave) * 1024u;
#pragma unroll
    for (int i = 0; i < 8; ++i)
      asm volatile("buffer_load_dwordx4 %0, %1, %2, %3 offen offset:%4" : "=v"(xr[i]) : "v"(vo + (i >> 2) * 4096), "s"(rX), "s"(so), "i"((i & 3) * 1024) : "memory");
    VMWAIT(8);
    dma_run4(rWQKV, vq, (unsigned)(64 * wave) * 3072u + (512u + 64u * c) * 2u, 8 * 3072, LB + L_WK + (unsigned)(64 * wave) * 128u, 2);
    asm volatile("s_waitcnt vmcnt(%9) lgkmcnt(0)" : "+v"(xr[0]), "+v"(xr[1]), "+v"(xr[2]), "+v"(xr[3]), "+v"(xr[4]), "+v"(xr[5]), "+v"(xr[6]), "+v"(xr[7]), "+s"(T) : "i"(8) : "memory");
    T += 1ull;
    STAMP(1);
    ln8(xr, LB + L_XLN + (unsigned)(wave >> 2) * 32768u + (unsigned)(lane >> 1) * 1024u + (unsigned)(lane & 1) * 512u + (unsigned)(wave & 3) * 128u, lane);
    STAMP(30);
  }
  const int Hp = H - 8 + wave;

  if (wave < 4) {
    dma_run4(rWQKV, vq, (unsigned)(64 * wave) * 3072u + (1024u + 64u * c) * 2u, 8 * 3072, LB + L_WV + (unsigned)(64 * wave) * 128u, 2);
    VMWAIT(8);
    BAR();
    STAMP(2);
    const unsigned trn = LB + (unsigned)((8 * h + trq) * 128 + ((hp ^ (trq >> 1)) * 64) + trcb * 32 + trp * 8);
    unsigned trn2 = trn + 65536u, trn3 = trn + L_WQA;
    OPQ(trn2); OPQ(trn3);
    bf16x8 xf[32];
    const unsigned xb = LB + L_XLN + (unsigned)tau * 32768u + (unsigned)h * 512u;
    auto ldx = [&](int s) { xf[s] = ldsfrag(xb + (unsigned)s * 1024u + (unsigned)((r ^ ((2 * (s & 3) + h) & 7)) << 4)); };
    f32x16 aK, aV, aQ;
#pragma unroll
    for (int i = 0; i < 16; ++i) { aK[i] = 0.f; aV[i] = 0.f; aQ[i] = 0.f; }
    u32x4 kf0, kf1, vf0, vf1;
#pragma unroll
    for (int s = 0; s < 4; ++s) ldx(s);
    const unsigned kvo = (unsigned)((tt * 8 + c) * 4 + hp * 2) * 1024u + lane * 16;
    pipe<32, 4>([&](auto I_) __attribute__((always_inline)) { constexpr int i = I_.value; return trfrag(trn + i * 2048, trn + i * 2048 + 512); },
                [&](auto I_, const bf16x8& w) __attribute__((always_inline)) {
                  constexpr int i = I_.value;
                  MFMAP(aK, w, xf[i]);
                  if constexpr (i + 4 < 32) ldx(i + 4);
                },
                [&](auto) {});
    kf0 = pack8(aK, 0); kf1 = pack8(aK, 8);
    __builtin_amdgcn_raw_buffer_store_b128(kf0, rWS, OFF_K + kvo, 0, 16);
    __builtin_amdgcn_raw_buffer_store_b128(kf1, rWS, OFF_K + kvo + 1024, 0, 16);
    STAMP(3);
    VMWAIT(0);
    if (lane == 0) __hip_atomic_store((GAS u64*)(ws + OFF_KF + (unsigned)(H * 8 + c) * 128u) + wave, T, RLX_AGENT);
    RBAR();
    STAMP(27);
    pipe<32, 5>([&](auto I_) __attribute__((always_inline)) { constexpr int i = I_.value; return trfrag(trn2 + i * 2048, trn2 + i * 2048 + 512); },
                [&](auto I_, const bf16x8& w) __attribute__((always_inline)) { MFMAP(aV, xf[I_.value], w); },
                [&](auto I_) __attribute__((always_inline)) { if (I_.value == 16) RBAR();   });
    vf0 = pack8(aV, 0); vf1 = pack8(aV, 8);
    __builtin_amdgcn_raw_buffer_store_b128(vf0, rWS, OFF_V + kvo, 0, 16);
    __builtin_amdgcn_raw_buffer_store_b128(vf1, rWS, OFF_V + kvo + 1024, 0, 16);
    STAMP(4);
    RBAR();
    pipe<32, 5>(
        [&](auto I_) __attribute__((always_inline)) {
          constexpr int i = I_.value;
          const unsigned a = i < 16 ? trn3 + i * 2048 : trn + L_WQB + (i - 16) * 2048;
          return trfrag(a, a + 512);
        },
        [&](auto I_, const bf16x8& w) __attribute__((always_inline)) {
          constexpr int i = I_.value;
          MFMAP(aQ, w, xf[i]);
          if (i == 18) {
            VMWAIT(0);
            if (lane == 0) __hip_atomic_store((GAS u64*)(ws + OFF_VF + (unsigned)(H * 8 + c) * 128u) + wave, T, RLX_AGENT);
          }
        },
        [&](auto) {});
    STAMP(5);
    RBAR();
#pragma unroll
    for (int i = 0; i < 16; ++i) aQ[i] *= 0.25f * 1.44269504088896f;
    qf = pack8(aQ, 0);
    const u32x4 qf1 = pack8(aQ, 8);
    ldsst(LB + L_K + (unsigned)((8 + tau) * 4 + hp * 2) * 1024u + lane * 16, kf0);
    ldsst(LB + L_K + (unsigned)((8 + tau) * 4 + hp * 2 + 1) * 1024u + lane * 16, kf1);
    ldsst(LB + L_V + (unsigned)((8 + tau) * 4 + hp * 2) * 1024u + lane * 16, vf0);
    ldsst(LB + L_V + (unsigned)((8 + tau) * 4 + hp * 2 + 1) * 1024u + lane * 16, vf1);
    ldsst(LB + L_QH + (unsigned)wave * 1024u + lane * 16, qf1);
    { const u32x4 one4 = {0x3F803F80u, 0x3F803F80u, 0x3F803F80u, 0x3F803F80u}; ldsst(LB + L_ONES, one4); }
#pragma unroll
    for (int g = 0; g < 4; ++g)
      xres[g] = __builtin_amdgcn_raw_buffer_load_b64(rX, (unsigned)((32 * tt + r) * 1024 + (64 * c + 32 * hp + 8 * g + 4 * h) * 2), 0, 0);
    BAR();
  } else {
    const int hw = wave - 4;
    VMWAIT(0);
    BAR();
    if (j == 0 && hw == 0 && lane == 0) __hip_atomic_store((GAS u64*)(ws + OFF_READY + (unsigned)G * 256u), T, RLX_AGENT);
    u32x4 st[16];
#pragma unroll
    for (int i = 0; i < 16; ++i) {
      const unsigned so = (unsigned)((i < 8 ? 256 : 0) + 64 * hw + 8 * (i & 7)) * 3072u + ((i < 8 ? 1024u : 0u) + 64u * c) * 2u;
      asm volatile("buffer_load_dwordx4 %0, %1, %2, %3 offen" : "=v"(st[i]) : "v"(vq), "s"(rWQKV), "s"(so) : "memory");
    }
    unsigned pfd;
    {
      const int n = hw == 3 ? 16 : 43;
      const unsigned po = (unsigned)(b * n + (lane < n ? lane : 0)) * 128u;
      const rsrc_t rp = hw == 0 ? rWG : hw == 1 ? rWU : hw == 2 ? rWD : rWO;
      asm volatile("buffer_load_dword %0, %1, %2, 0 offen" : "=v"(pfd) : "v"(po), "s"(rp) : "memory");
    }
    STAMP(3);
    RBAR();
    dma_run4(rWQKV, vq, (unsigned)(256 + 64 * hw) * 3072u + (64u * c) * 2u, 8 * 3072, LB + L_WQB + (unsigned)(64 * hw) * 128u, 2);
    asm volatile("s_waitcnt vmcnt(17)" : "+v"(st[0]), "+v"(st[1]), "+v"(st[2]), "+v"(st[3]), "+v"(st[4]), "+v"(st[5]), "+v"(st[6]), "+v"(st[7])::"memory");
#pragma unroll
    for (int i = 0; i < 8; ++i) ldsst(LB + L_WV + (unsigned)(256 + 64 * hw + 8 * i) * 128u + lane * 16, st[i]);
    BAR();
    asm volatile("s_waitcnt vmcnt(9)" : "+v"(st[8]), "+v"(st[9]), "+v"(st[10]), "+v"(st[11]), "+v"(st[12]), "+v"(st[13]), "+v"(st[14]), "+v"(st[15])::"memory");
#pragma unroll
    for (int i = 0; i < 8; ++i) ldsst(LB + L_WQA + (unsigned)(64 * hw + 8 * i) * 128u + lane * 16, st[8 + i]);
    asm volatile("s_waitcnt vmcnt(0)" : "+v"(pfd)::"memory");
    BAR();
    if (Hp >= 0) {
      poll_tag((const GAS u64*)(ws + OFF_KF + (unsigned)(Hp * 8 + c) * 128u) + (lane & 3), T);
      dma_run4_sc1(rWS, lane * 16, OFF_K + (unsigned)((2 * Hp) * 8 + c) * 4096u, LB + L_K + (unsigned)(8 * hw) * 1024u);
      dma_run4_sc1(rWS, lane * 16, OFF_K + (unsigned)((2 * Hp + 1) * 8 + c) * 4096u, LB + L_K + (unsigned)(8 * hw + 4) * 1024u);
    }
    STAMP(5);
    RBAR();
    VMWAIT(0);
    RBAR();
    qf = *(LAS u32x4*)(LB + L_QH + (unsigned)hw * 1024u + lane * 16);
#pragma unroll
    for (int g = 0; g < 4; ++g) { xres[g][0] = 0u; xres[g][1] = 0u; }
  }
  STAMP(6);

  const int du0 = (43 * j) >> 4, ndu = ((43 * (j + 1)) >> 4) - du0;
  unsigned v3g[3], v3u[3];
#pragma unroll
  for (int i = 0; i < 3; ++i) {
    const unsigned qd = (unsigned)(i * 64 + lane), row = qd / 12u, ch = qd - row * 12u;
    v3g[i] = row * 2752u + ch * 16u;
    v3u[i] = row * 2752u + (ch ^ 2u) * 16u;
  }
  {
    const int e = wave >> 2;
    const unsigned ka = LB + L_K + (unsigned)(tau * 4 + hp * 2 + e) * 1024u + lane * 16;
    const unsigned va = LB + L_V + (unsigned)(tau * 4 + hp * 2) * 1024u + lane * 16;
    const unsigned so0 = (unsigned)(16 * wave) * 2752u + 64u * du0, ld0 = LB + (unsigned)(16 * wave) * 192u;
    auto hook = [&](int n) {
      if (n < 3) { dma16(rWG, v3g[n], so0, ld0 + n * 1024u); dma16(rWU, v3u[n], so0, ld0 + 24576u + n * 1024u); }
      else if (n >= 8) {
        const unsigned row0 = (unsigned)(64 * wave + 16 * (n - 8));
        dma16(rWO, v1k, row0 * 1024u + 128u * c, LB + L_WO + row0 * 128u);
        dma16(rWO, v1k, (row0 + 8u) * 1024u + 128u * c, LB + L_WO + (row0 + 8u) * 128u);
      }
    };
    auto mid = [&]() {
      if (wave >= 4) {
        const int hw = wave - 4;
        poll_tag((lane == 4 || Hp < 0) ? (const GAS u64*)(ws + OFF_READY + (unsigned)G * 256u) : (const GAS u64*)(ws + OFF_VF + (unsigned)(Hp * 8 + c) * 128u) + (lane & 3), T);
        if (Hp >= 0) {
          dma_run4_sc1(rWS, lane * 16, OFF_V + (unsigned)((2 * Hp) * 8 + c) * 4096u, LB + L_V + (unsigned)(8 * hw) * 1024u);
          dma_run4_sc1(rWS, lane * 16, OFF_V + (unsigned)((2 * Hp + 1) * 8 + c) * 4096u, LB + L_V + (unsigned)(8 * hw + 4) * 1024u);
        }
      }
    };
    auto st = [&](int i) { STAMP(i); };
    u32x4 of;
    if (tt >= 8) of = attention<true>(ka, va, asfrag(qf), 0, e, r, h, wave >= 4, mid, hook, st);
    else of = attention<false>(ka, va, asfrag(qf), 8 - tt, e, r, h, wave >= 4, mid, hook, st);
    __builtin_amdgcn_raw_buffer_store_b128(of, rWS, OFF_O + (unsigned)(tt * 32 + 4 * c + 2 * hp + e) * 1024u + lane * 16, 0, 16);
  }
  STAMP(17);
  const unsigned trpm = (unsigned)((4 * h + trq) * 128 + trcb * 32 + trp * 8);

  if (wave < 4) {
    const int kh = hp;
    VMWAIT(0);
    const unsigned haveO = arrive(cntT, lane);
    RBAR();
    STAMP(8);
    wait_cnt(cntT, haveO, 32u);
    STAMP(9);
    u32x4 of[16];
#pragma unroll
    for (int s = 0; s < 16; ++s) of[s] = __builtin_amdgcn_raw_buffer_load_b128(rWS, OFF_O + (unsigned)(tt * 32 + 16 * kh + s) * 1024u + lane * 16, 0, 16);
    const unsigned aA = LB + L_WO + (unsigned)kh * 32768u + trpm + (unsigned)((kh ^ (trq >> 1)) * 64);
    const unsigned aB = aA ^ 64u;
    f32x16 accA, accB;
#pragma unroll
    for (int i = 0; i < 16; ++i) { accA[i] = 0.f; accB[i] = 0.f; }
    pipe<32, 6>([&](auto I_) { constexpr int i = I_.value; const unsigned a = ((i & 1) ? aA : aB) + (i >> 1) * 2048; return trfrag(a, a + 1024); },
                [&](auto I_, const bf16x8& w) { constexpr int i = I_.value; if (i & 1) MFMAP(accA, w, asfrag(of[i >> 1])); else MFMAP(accB, w, asfrag(of[i >> 1])); },
                [&](auto) {});
    STAMP(18);
    RBAR();
#pragma unroll
    for (int g = 0; g < 4; ++g) { f32x4 v = {accB[4 * g], accB[4 * g + 1], accB[4 * g + 2], accB[4 * g + 3]}; *(LAS f32x4*)(LB + L_SCR + (unsigned)wave * 4096u + g * 1024 + lane * 16) = v; }
    BAR();
    unsigned hres[8];
    {
      float hv[16];
#pragma unroll
      for (int g = 0; g < 4; ++g) {
        const f32x4 v = *(LAS f32x4*)(LB + L_SCR + (unsigned)(wave ^ 1) * 4096u + g * 1024 + lane * 16);
        hres[2 * g] = pk2(accA[4 * g] + v[0] + blo(xres[g][0]), accA[4 * g + 1] + v[1] + bhi(xres[g][0]));
        hres[2 * g + 1] = pk2(accA[4 * g + 2] + v[2] + blo(xres[g][1]), accA[4 * g + 3] + v[3] + bhi(xres[g][1]));
      }
      const unsigned o = OFF_H + (unsigned)(tt * 32 + 4 * c + 2 * kh) * 1024u + lane * 16;
      u32x4 h0 = {hres[0], hres[1], hres[2], hres[3]}, h1 = {hres[4], hres[5], hres[6], hres[7]};
      __builtin_amdgcn_raw_buffer_store_b128(h0, rWS, o, 0, 16);
      __builtin_amdgcn_raw_buffer_store_b128(h1, rWS, o + 1024, 0, 16);
      float sum = 0.f;
#pragma unroll
      for (int i = 0; i < 8; ++i) { hv[2 * i] = blo(hres[i]); hv[2 * i + 1] = bhi(hres[i]); sum += hv[2 * i] + hv[2 * i + 1]; }
      const float mean = swap32_add(sum) * (1.f / 32.f);
      float m2 = 0.f;
#pragma unroll
      for (int i = 0; i < 16; ++i) { const float d = hv[i] - mean; m2 = fmaf(d, d, m2); }
      m2 = swap32_add(m2);
      u32x2 st = {__float_as_uint(mean), __float_as_uint(m2)};
      if (h == 0) __builtin_amdgcn_raw_buffer_store_b64(st, rWS, OFF_HS + (unsigned)((tt * 16 + 2 * c + kh) * 256 + r * 8), 0, 16);
    }
    STAMP(19);
    VMWAIT(0);
    RBAR();
    (void)arrive(cntT + 32, lane);
    STAMP(10);

    const int t4 = 4 * G + wave;
    GAS unsigned* cnt4 = (GAS unsigned*)(ws + OFF_CNT + (unsigned)t4 * 1024u);
    wait_cnt(cnt4 + 32, 0u, 16u);
    STAMP(11);
    const unsigned hb = OFF_H + (unsigned)t4 * 32768u + lane * 16;
    u32x4 hf[32];
#pragma unroll
    for (int s = 0; s < 12; ++s) hf[s] = __builtin_amdgcn_raw_buffer_load_b128(rWS, hb + s * 1024, 0, 16);
    u32x2 stp[8];
    const unsigned w3e = LB + (unsigned)(trcb * 24576 + (4 * h + trq) * 192 + trp * 8 + trcb * 32);
    const unsigned w3o = LB + (unsigned)(trcb * 24576 + (4 * h + trq) * 192 + trp * 8 - trcb * 32);
    unsigned w3e1 = w3e + 49152u, w3o1 = w3o + 49152u, w3e2 = w3e + 98304u, w3o2 = w3o + 98304u, csa = LB + L_SCR + (unsigned)r * 16u;
    OPQ(w3e1); OPQ(w3o1); OPQ(w3e2); OPQ(w3o2); OPQ(csa);
    f32x16 ac[6];
#pragma unroll
    for (int u = 0; u < 6; ++u)
#pragma unroll
      for (int i = 0; i < 16; ++i) ac[u][i] = 0.f;
    float rsg, irs;
    u32x4 mfr;
    float tg[8];
    const unsigned hho = OFF_HH + (unsigned)(t4 * 86 + 2 * du0) * 1024u + lane * 16;
    const unsigned hho2 = ndu == 3 ? hho + 4096u : 0x7F000000u;
    auto epi = [&](int u, int k) {
      if (k == 1) {
#pragma unroll
        for (int i = 0; i < 8; ++i) tg[i] = ac[u][i] * rsg;
      } else if (k == 2 || k == 3) {
#pragma unroll
        for (int i = 4 * (k - 2); i < 4 * (k - 1); ++i) tg[i] = __builtin_amdgcn_exp2f(tg[i]);
      } else if (k == 4) {
#pragma unroll
        for (int i = 0; i < 8; ++i) tg[i] = fmaf(tg[i], irs, irs);
      } else if (k == 5 || k == 6) {
#pragma unroll
        for (int i = 4 * (k - 5); i < 4 * (k - 4); ++i) tg[i] = __builtin_amdgcn_rcpf(tg[i]);
      } else if (k == 7) {
#pragma unroll
        for (int i = 0; i < 8; ++i) tg[i] *= ac[u][8 + i];
      } else if (k == 8) {
        u32x4 o = {pk2(tg[0], tg[1]), pk2(tg[2], tg[3]), pk2(tg[4], tg[5]), pk2(tg[6], tg[7])};
        __builtin_amdgcn_raw_buffer_store_b128(o, rWS, u < 4 ? hho + u * 1024 : hho2 + (u - 4) * 1024, 0, 16);
      }
      if (k >= 1 && k <= 7) {
#pragma unroll
        for (int i = 0; i < 8; ++i) OPQ(tg[i]);
      }
    };
    pipe<198, 6>(
        [&](auto I_) __attribute__((always_inline)) {
          constexpr int i = I_.value;
          if constexpr (i < 144) {
            constexpr int u = i % 6, s = (i % 48) / 6;
            const unsigned a = ((i / 48 == 1) ? ((u & 1) ? w3o1 : w3e1) : ((u & 1) ? w3o : w3e)) + s * 3072 + u * 32;
            return trfrag(a, a + 1536);
          } else {
            constexpr int u = (i - 144) / 9, s = (i - 144) % 9;
            if constexpr (s == 8) return ldsfrag(csa + u * 512);
            else { const unsigned a = ((u & 1) ? w3o2 : w3e2) + s * 3072 + u * 32; return trfrag(a, a + 1536); }
          }
        },
        [&](auto I_, const bf16x8& w) __attribute__((always_inline)) {
          constexpr int i = I_.value;
          if constexpr (i < 144) {
            constexpr int u = i % 6, ks = i / 6, s = ks < 16 ? ks : ks + 8;
            MFMAP(ac[u], w, asfrag(hf[s]));
            if constexpr (u == 5 && ks + 12 < 32) { constexpr int k2 = ks + 12, s2 = k2 < 16 ? k2 : k2 < 24 ? k2 + 8 : k2 - 8; hf[s2] = __builtin_amdgcn_raw_buffer_load_b128(rWS, hb + s2 * 1024, 0, 16); }
            if (i == 121) {
#pragma unroll
              for (int q = 0; q < 8; ++q) stp[q] = __builtin_amdgcn_raw_buffer_load_b64(rWS, OFF_HS + (unsigned)((t4 * 16 + 2 * q + h) * 256 + r * 8), 0, 16);
            }
          } else {
            constexpr int u = (i - 144) / 9, s = (i - 144) % 9;
            if constexpr (s == 8) MFMAP(ac[u], w, asfrag(mfr));
            else MFMAP(ac[u], w, asfrag(hf[16 + s]));
            if (i == 146) {
              float ms = 0.f, q = 0.f;
#pragma unroll
              for (int k = 0; k < 8; ++k) { ms += __uint_as_float(stp[k][0]); q += __uint_as_float(stp[k][1]); }
              const float mean = swap32_add(ms) * (1.f / 16.f);
#pragma unroll
              for (int k = 0; k < 8; ++k) { const float d = __uint_as_float(stp[k][0]) - mean; q = fmaf(32.f * d, d, q); }
              q = swap32_add(q) * (1.f / 512.f) + 1e-5f;
              const float rs = __builtin_amdgcn_rsqf(q);
              rsg = rs * -1.44269504088896f;
              irs = q * rs;
              const unsigned a1 = pk2(-mean, 0.f) & 0xFFFFu;
              const float r1 = -mean - blo(a1);
              const unsigned a2 = pk2(r1, 0.f) & 0xFFFFu;
              const unsigned a3 = pk2(r1 - blo(a2), 0.f) & 0xFFFFu;
              mfr[0] = h ? 0u : (a1 | (a2 << 16)); mfr[1] = h ? 0u : (a1 | (a3 << 16)); mfr[2] = h ? 0u : (a2 | (a1 << 16)); mfr[3] = 0u;
            }
            if constexpr (u > 0) epi(u - 1, s);
            if (i == 144 + 43) { VMWAIT(1); signal(cnt4 + 64, lane); }
          }
        },
        [&](auto I_) __attribute__((always_inline)) {
          constexpr int i = I_.value;
          if (i == 54) RBAR();
          if (i == 96) { STAMP(21); RBAR(); STAMP(22); }
          if (i == 150) { RBAR(); STAMP(23); }
        });
    STAMP(12);
#pragma unroll
    for (int k = 1; k < 9; ++k) epi(5, k);
    VMWAIT(2);
    signal(cnt4 + 96, lane);
    VMWAIT(1);
    if (ndu == 3) signal(cnt4 + 128, lane);
    RBAR();
    STAMP(13);

    const unsigned gb = OFF_HH + (unsigned)(tt * 86 + kh) * 1024u + lane * 16;
    u32x4 gf[43];
    wait_cnt(cntT + 64, 0u, 16u);
    STAMP(14);
#pragma unroll
    for (int m = 0; m < 16; ++m) gf[m] = __builtin_amdgcn_raw_buffer_load_b128(rWS, gb + Dm(m) * 2048, 0, 16);
    STAMP(24);
    RBAR();
    STAMP(25);
#pragma unroll
    for (int m = 16; m < 36; ++m) gf[m] = __builtin_amdgcn_raw_buffer_load_b128(rWS, gb + Dm(m) * 2048, 0, 16);
    const unsigned dA = LB + (unsigned)kh * 2048u + trpm + (unsigned)((kh ^ (trq >> 1)) * 64);
    const unsigned dB = dA ^ 64u;
    unsigned dA1 = dA + 49152u, dB1 = dB + 49152u, dA2 = dA + 98304u, dB2 = dB + 98304u;
    OPQ(dA1); OPQ(dB1); OPQ(dA2); OPQ(dB2);
#pragma unroll
    for (int i = 0; i < 16; ++i) { accA[i] = 0.f; accB[i] = 0.f; }
    pipe<86, 6>(
        [&](auto I_) __attribute__((always_inline)) {
          constexpr int i = I_.value;
          constexpr int m = i >> 1, p = m / 12, mm = m % 12;
          const unsigned a = ((i & 1) ? ((p == 0 || p == 3) ? dA1 : p == 1 ? dA : dA2) : ((p == 0 || p == 3) ? dB1 : p == 1 ? dB : dB2)) + mm * 4096;
          return trfrag(a, a + 1024);
        },
        [&](auto I_, const bf16x8& w) __attribute__((always_inline)) {
          constexpr int i = I_.value;
          if (i & 1) MFMAP(accA, w, asfrag(gf[i >> 1])); else MFMAP(accB, w, asfrag(gf[i >> 1]));
          if constexpr ((i & 1) && (i >> 1) < 7) gf[36 + (i >> 1)] = __builtin_amdgcn_raw_buffer_load_b128(rWS, gb + Dm(36 + (i >> 1)) * 2048, 0, 16);
        },
        [&](auto I_) __attribute__((always_inline)) {
          constexpr int i = I_.value;
          if (i == 30) RBAR();
          if (i == 48) RBAR();
          if (i == 72) RBAR();
        });
    STAMP(26);
    RBAR();
    {
      const unsigned ob = LB + (unsigned)kh * 17408u + (unsigned)(32 * tau + r) * 272u + (unsigned)h * 16u;
#pragma unroll
      for (int g = 0; g < 4; ++g) {
        const f32x4 va = {accA[4 * g] + blo(hres[2 * g]), accA[4 * g + 1] + bhi(hres[2 * g]), accA[4 * g + 2] + blo(hres[2 * g + 1]), accA[4 * g + 3] + bhi(hres[2 * g + 1])};
        const f32x4 vb = {accB[4 * g], accB[4 * g + 1], accB[4 * g + 2], accB[4 * g + 3]};
        *(LAS f32x4*)(ob + (unsigned)kh * 128u + g * 32) = va;
        *(LAS f32x4*)(ob + (unsigned)(kh ^ 1) * 128u + g * 32) = vb;
      }
    }
    BAR();
    STAMP(15);
  } else {
    const int hw = wave - 4;
    auto w3blk = [&](int blk, unsigned dst) {
#pragma unroll 1
      for (int p = 0; p < 2; ++p) {
        const unsigned row0 = (unsigned)(32 * hw + 16 * p), so = (128u * blk + row0) * 2752u + 64u * du0, ld = LB + dst + row0 * 192u;
#pragma unroll
        for (int i = 0; i < 3; ++i) {
          dma16(rWG, v3g[i], so, ld + i * 1024u);
          dma16(rWU, v3u[i], so, ld + 24576u + i * 1024u);
        }
      }
    };
    VMWAIT(0);
    signal(cntT, lane);
    RBAR();
    w3blk(1, 49152);
    u32x4 b3[12];
#pragma unroll
    for (int q = 0; q < 12; ++q) {
      const unsigned so = (384u + (unsigned)(32 * hw + 16 * (q / 6))) * 2752u + 64u * du0;
      if ((q % 6) < 3) asm volatile("buffer_load_dwordx4 %0, %1, %2, %3 offen" : "=v"(b3[q]) : "v"(v3g[q % 3]), "s"(rWG), "s"(so) : "memory");
      else asm volatile("buffer_load_dwordx4 %0, %1, %2, %3 offen" : "=v"(b3[q]) : "v"(v3u[q % 3]), "s"(rWU), "s"(so) : "memory");
    }
    VMWAIT(12);
    RBAR();
    RBAR();
    RBAR();
    w3blk(2, 98304);
    asm volatile("s_waitcnt vmcnt(0)" : "+v"(b3[0]), "+v"(b3[1]), "+v"(b3[2]), "+v"(b3[3]), "+v"(b3[4]), "+v"(b3[5]), "+v"(b3[6]), "+v"(b3[7]), "+v"(b3[8]), "+v"(b3[9]), "+v"(b3[10]), "+v"(b3[11])::"memory");
    if (hw == (G & 3)) {
      const int blk = G >> 2;
      if (blk == 3) {
#pragma unroll
        for (int q = 0; q < 12; ++q) ldsst(LB + L_SCR + (unsigned)(16 * (q / 6)) * 192u + ((q % 6) >= 3 ? 6144u : 0u) + (unsigned)(q % 3) * 1024u + lane * 16, b3[q]);
        asm volatile("s_waitcnt lgkmcnt(0)" ::: "memory");
      }
      const unsigned we = LB + (blk == 3 ? L_SCR + (unsigned)trcb * 6144u : (unsigned)(blk * 49152 + trcb * 24576 + 32 * hw * 192)) + (unsigned)((4 * h + trq) * 192 + trp * 8 + trcb * 32);
      const unsigned wo = we - (unsigned)(trcb * 64);
      f32x16 cs;
#pragma unroll
      for (int i = 0; i < 16; ++i) cs[i] = 0.f;
      bf16x8 f[12];
#pragma unroll
      for (int q = 0; q < 12; ++q) { const int u = q >> 1; const unsigned a = ((u & 1) ? wo : we) + (q & 1) * 3072 + u * 32; f[q] = trfrag(a, a + 1536); }
#pragma unroll
      for (int q = 0; q < 12; ++q) {
        const unsigned sel = (r == (q >> 1)) ? 0x3F803F80u : 0u;
        const u32x4 sv = {sel, sel, sel, sel};
        cs = MFMA(f[q], asfrag(sv), cs);
      }
      if (r < 6) {
        const unsigned tg32 = (unsigned)T;
#pragma unroll
        for (int g = 0; g < 16; ++g)
          __hip_atomic_store((GAS u64*)(ws + OFF_CS2 + (unsigned)((j * 16 + G) * 1536 + (r * 32 + (g >> 2) * 8 + h * 4 + (g & 3)) * 8)), ((u64)tg32 << 32) | __float_as_uint(cs[g]), RLX_AGENT);
      }
    }
    if (b == 0 && hw == 3) {
      unsigned spins = 0;
#pragma unroll 1
      for (;;) {
        bool ok = true;
#pragma unroll
        for (int i = 0; i < 4; ++i) ok &= __hip_atomic_load((const GAS u64*)(ws + OFF_KF + (unsigned)(i * 64 + lane) * 128u), RLX_AGENT) == T;
        if (__all(ok)) break;
        __builtin_amdgcn_s_sleep(2);
        if (++spins > SPIN_CAP) break;
      }
      if (lane == 0) __hip_atomic_store((GAS u64*)(ws + OFF_EPOCH), T, RLX_AGENT);
    }
    STAMP(20);
    RBAR();
#pragma unroll
    for (int q = 0; q < 12; ++q)
      ldsst(LB + (unsigned)(32 * hw + 16 * (q / 6)) * 192u + ((q % 6) >= 3 ? 24576u : 0u) + (unsigned)(q % 3) * 1024u + lane * 16, b3[q]);
    STAMP(21);
    BAR();
    if (hw < 3) {
      float pv[16];
      unsigned spins = 0;
#pragma unroll 1
      for (;;) {
        bool ok = true;
#pragma unroll
        for (int g = 0; g < 16; ++g) {
          const u64 x = __hip_atomic_load((const GAS u64*)(ws + OFF_CS2 + (unsigned)((j * 16 + g) * 1536 + (hw * 64 + lane) * 8)), RLX_AGENT);
          pv[g] = __uint_as_float((unsigned)x);
          ok &= (unsigned)(x >> 32) == (unsigned)T;
        }
        if (__all(ok)) break;
        __builtin_amdgcn_s_sleep(2);
        if (++spins > SPIN_CAP) break;
      }
      const float v = (((pv[0] + pv[1]) + (pv[2] + pv[3])) + ((pv[4] + pv[5]) + (pv[6] + pv[7]))) + (((pv[8] + pv[9]) + (pv[10] + pv[11])) + ((pv[12] + pv[13]) + (pv[14] + pv[15])));
      const unsigned c1 = pk2(v, 0.f) & 0xFFFFu;
      const float r1 = v - blo(c1);
      const unsigned c2 = pk2(r1, 0.f) & 0xFFFFu;
      const unsigned c3 = pk2(r1 - blo(c2), 0.f) & 0xFFFFu;
      const u32x4 f = {c1 | (c1 << 16), c2 | (c1 << 16), c2 | (c3 << 16), 0u};
      ldsst(LB + L_SCR + (unsigned)(hw * 64 + lane) * 16u, f);
    }
    BAR();
    auto w4 = [&](int m0, int n, unsigned dst) {
#pragma unroll 1
      for (int q = 0; q < n; ++q) {
        const int mm = hw + 4 * q;
        dma_run4(rWD, v1k, (unsigned)Dm(m0 + mm) * 32768u + 128u * c, 8192, LB + dst + (unsigned)mm * 4096u, 1);
      }
    };
    w4(0, 3, 49152);
    w4(12, 3, 0);
    RBAR();
    w4(24, 3, 98304);
    wait_cnt(cntT + 96 + 32 * hp, 0u, hp ? 11u : 16u);
    VMWAIT(0);
    RBAR();
    RBAR();
    w4(36, 2, 49152);
    VMWAIT(8);
    RBAR();
    VMWAIT(0);
    RBAR();
    RBAR();
    RBAR();
  }
  {
    const int row = threadIdx.x >> 3, ch = threadIdx.x & 7;
    const unsigned a = LB + (unsigned)row * 272u + (unsigned)ch * 32u;
    const f32x4 p0 = *(LAS f32x4*)a + *(LAS f32x4*)(a + 17408), p1 = *(LAS f32x4*)(a + 16) + *(LAS f32x4*)(a + 17408 + 16);
    const u32x4 o = {pk2(p0[0], p0[1]), pk2(p0[2], p0[3]), pk2(p1[0], p1[1]), pk2(p1[2], p1[3])};
    __builtin_amdgcn_raw_buffer_store_b128(o, rOUT, (unsigned)((64 * H + row) * 1024 + 128 * c + ch * 16), 0, 16);
  }
  STAMP_END();
}

extern "C" void kernel_launch(void* const* d_in, const int* in_sizes, int n_in, void* d_out, int out_size, void* d_ws, size_t ws_size,
                              hipStream_t stream) {
  (void)in_sizes; (void)n_in; (void)out_size; (void)ws_size;
  (void)hipFuncSetAttribute((const void*)fused_block, hipFuncAttributeMaxDynamicSharedMemorySize, (int)LDS_BYTES);
  hipLaunchKernelGGL(fused_block, dim3(256), dim3(512), LDS_BYTES, stream, d_in[0], d_in[1], d_in[2], d_in[4], d_in[5], d_in[6], d_out, (char*)d_ws);
}
```

```cpp
#include <hip/hip_runtime.h>
#include <hip/hip_bf16.h>
#include <utility>

typedef __bf16 bf16x8 __attribute__((ext_vector_type(8)));
typedef __bf16 bf16x4 __attribute__((ext_vector_type(4)));
typedef __bf16 bf16x2 __attribute__((ext_vector_type(2)));
typedef float f32x16 __attribute__((ext_vector_type(16)));
typedef float f32x4 __attribute__((ext_vector_type(4)));
typedef float f32x2 __attribute__((ext_vector_type(2)));
typedef unsigned u32x4 __attribute__((ext_vector_type(4)));
typedef unsigned u32x2 __attribute__((ext_vector_type(2)));
typedef unsigned long long u64;
#define LAS __attribute__((address_space(3)))
#define GAS __attribute__((address_space(1)))
#define DI __device__ __forceinline__
#define RLX_AGENT __ATOMIC_RELAXED, __HIP_MEMORY_SCOPE_AGENT
#define MFMA(a, b, c) __builtin_amdgcn_mfma_f32_32x32x16_bf16((a), (b), (c), 0, 0, 0)
#define MFMAP(acc, a, b) do { acc = MFMA(a, b, acc); asm volatile("" : "+v"(acc)); } while (0)
#define OPQ(x) asm volatile("" : "+v"(x))
typedef __amdgpu_buffer_rsrc_t rsrc_t;

constexpr unsigned OFF_EPOCH = 0;
constexpr unsigned OFF_READY = 4096;
constexpr unsigned OFF_KF = 16384;
constexpr unsigned OFF_VF = OFF_KF + 32768;
constexpr unsigned OFF_CNT = OFF_VF + 32768;
constexpr unsigned OFF_CS2 = 9u * (1u << 20) + (512u << 10);
constexpr unsigned OFF_STAMP = 512u << 10;
constexpr unsigned OFF_K = 1u << 20;
constexpr unsigned OFF_V = 3u << 20;
constexpr unsigned OFF_O = 5u << 20;
constexpr unsigned OFF_H = 7u << 20;
constexpr unsigned OFF_HS = 9u << 20;
constexpr unsigned OFF_HH = 10u << 20;
constexpr unsigned WS_BYTES = 16u << 20;
constexpr unsigned KB = 1024;
constexpr unsigned L_WK = 0, L_WV = 64 * KB, L_XLN = 96 * KB, L_WQA = 128 * KB, L_WQB = 32 * KB;
constexpr unsigned L_K = 0, L_V = 48 * KB, L_QH = 40 * KB, L_ONES = 88 * KB, L_WO = 96 * KB;
constexpr unsigned L_SCR = 144 * KB;
constexpr unsigned LDS_BYTES = 160 * KB;
constexpr unsigned SPIN_CAP = 1u << 18;

#define BAR() do { asm volatile("s_waitcnt lgkmcnt(0)" ::: "memory"); __builtin_amdgcn_s_barrier(); asm volatile("" ::: "memory"); } while (0)
#define VMWAIT(n) asm volatile("s_waitcnt vmcnt(" #n ")" ::: "memory")

#define STAMP(i) do {} while (0)
#define STAMP_END() do {} while (0)

DI unsigned rfl(unsigned x) { return (unsigned)__builtin_amdgcn_readfirstlane((int)x); }
DI void dma16(rsrc_t rs, unsigned voff, unsigned soff, unsigned lds) {
  asm volatile("s_mov_b32 m0, %0\n\ts_nop 0\n\tbuffer_load_dwordx4 %1, %2, %3 offen lds" ::"s"(lds), "v"(voff), "s"(rs), "s"(soff) : "m0", "memory");
}
DI void dma16_sc1(rsrc_t rs, unsigned voff, unsigned soff, unsigned lds) {
  asm volatile("s_mov_b32 m0, %0\n\ts_nop 0\n\tbuffer_load_dwordx4 %1, %2, %3 offen sc1 lds" ::"s"(lds), "v"(voff), "s"(rs), "s"(soff) : "m0", "memory");
}
DI void dma_run4(rsrc_t rs, unsigned voff, unsigned soff, unsigned step, unsigned lds, int n4) {
#pragma unroll 1
  for (int i = 0; i < n4; ++i) {
    asm volatile(
        "s_mov_b32 m0, %[l]\n\ts_nop 0\n\tbuffer_load_dwordx4 %[v], %[r], %[s] offen lds\n\t"
        "s_add_u32 m0, m0, 0x400\n\ts_add_u32 %[s], %[s], %[st]\n\tbuffer_load_dwordx4 %[v], %[r], %[s] offen lds\n\t"
        "s_add_u32 m0, m0, 0x400\n\ts_add_u32 %[s], %[s], %[st]\n\tbuffer_load_dwordx4 %[v], %[r], %[s] offen lds\n\t"
        "s_add_u32 m0, m0, 0x400\n\ts_add_u32 %[s], %[s], %[st]\n\tbuffer_load_dwordx4 %[v], %[r], %[s] offen lds\n\t"
        "s_add_u32 %[s], %[s], %[st]\n\ts_add_u32 %[l], %[l], 0x1000"
        : [s] "+s"(soff), [l] "+s"(lds) : [v] "v"(voff), [r] "s"(rs), [st] "s"(step) : "m0", "scc", "memory");
  }
}
DI void dma_run4_sc1(rsrc_t rs, unsigned voff, unsigned soff, unsigned lds) {
  asm volatile(
      "s_mov_b32 m0, %[l]\n\ts_nop 0\n\tbuffer_load_dwordx4 %[v], %[r], %[s] offen sc1 lds\n\t"
      "buffer_load_dwordx4 %[v], %[r], %[s] offen offset:1024 sc1 lds\n\t"
      "buffer_load_dwordx4 %[v], %[r], %[s] offen offset:2048 sc1 lds\n\t"
      "buffer_load_dwordx4 %[v], %[r], %[s] offen offset:3072 sc1 lds"
      ::[s] "s"(soff), [l] "s"(lds), [v] "v"(voff), [r] "s"(rs) : "m0", "memory");
}
#define RBAR() do { asm volatile("" ::: "memory"); __builtin_amdgcn_s_barrier(); asm volatile("" ::: "memory"); } while (0)
#define SCHED() __builtin_amdgcn_sched_barrier(0)
template <int... I, class F> DI void sfor_(std::integer_sequence<int, I...>, F&& f) { (f(std::integral_constant<int, I>{}), ...); }
template <int N, class F> DI void sfor(F&& f) { sfor_(std::make_integer_sequence<int, N>{}, f); }
template <int N, int D, class LD, class MM, class PRE>
DI void pipe(LD&& ld, MM&& mm, PRE&& pre) {
  bf16x8 buf[D];
  sfor<D>([&](auto I) { pre(I); buf[I.value] = ld(I); });
  SCHED();
  sfor<N>([&](auto S) {
    constexpr int s = S.value;
    mm(S, buf[s % D]);
    if constexpr (s + D < N) { pre(std::integral_constant<int, s + D>{}); buf[s % D] = ld(std::integral_constant<int, s + D>{}); }
    SCHED();
  });
}
DI bf16x8 trfrag(unsigned a0, unsigned a1) {
  bf16x4 lo = __builtin_amdgcn_ds_read_tr16_b64_v4bf16((LAS bf16x4*)a0);
  bf16x4 hi = __builtin_amdgcn_ds_read_tr16_b64_v4bf16((LAS bf16x4*)a1);
  return __builtin_shufflevector(lo, hi, 0, 1, 2, 3, 4, 5, 6, 7);
}
DI bf16x8 ldsfrag(unsigned a) { return *(LAS bf16x8*)a; }
DI void ldsst(unsigned a, u32x4 v) { *(LAS u32x4*)a = v; }
DI unsigned pk2(float a, float b) { f32x2 v = {a, b}; return __builtin_bit_cast(unsigned, __builtin_convertvector(v, bf16x2)); }
DI u32x4 pack8(const f32x16& x, int o) { u32x4 r; r[0] = pk2(x[o], x[o + 1]); r[1] = pk2(x[o + 2], x[o + 3]); r[2] = pk2(x[o + 4], x[o + 5]); r[3] = pk2(x[o + 6], x[o + 7]); return r; }
DI float blo(unsigned u) { return __uint_as_float(u << 16); }
DI float bhi(unsigned u) { return __uint_as_float(u & 0xffff0000u); }
DI bf16x8 asfrag(u32x4 v) { return __builtin_bit_cast(bf16x8, v); }
DI float swap32_add(float a) {
  auto r = __builtin_amdgcn_permlane32_swap(__float_as_uint(a), __float_as_uint(a), false, false);
  return __uint_as_float(r[0]) + __uint_as_float(r[1]);
}
DI float swap32_max(float a) {
  auto r = __builtin_amdgcn_permlane32_swap(__float_as_uint(a), __float_as_uint(a), false, false);
  return fmaxf(__uint_as_float(r[0]), __uint_as_float(r[1]));
}
template <int CTRL> DI float dpp_add(float a) { return a + __uint_as_float(__builtin_amdgcn_update_dpp(0, __float_as_uint(a), CTRL, 0xf, 0xf, true)); }
DI float row16_sum(float a) { a = dpp_add<0x128>(a); a = dpp_add<0x124>(a); a = dpp_add<0x122>(a); a = dpp_add<0x121>(a); return a; }
DI void red8(const float (&v)[8], float& A, float& B) {
  float t[4];
#pragma unroll
  for (int i = 0; i < 4; ++i) {
    auto r = __builtin_amdgcn_permlane32_swap(__float_as_uint(v[2 * i]), __float_as_uint(v[2 * i + 1]), false, false);
    t[i] = __uint_as_float(r[0]) + __uint_as_float(r[1]);
  }
  auto r0 = __builtin_amdgcn_permlane16_swap(__float_as_uint(t[0]), __float_as_uint(t[1]), false, false);
  auto r1 = __builtin_amdgcn_permlane16_swap(__float_as_uint(t[2]), __float_as_uint(t[3]), false, false);
  A = row16_sum(__uint_as_float(r0[0]) + __uint_as_float(r0[1]));
  B = row16_sum(__uint_as_float(r1[0]) + __uint_as_float(r1[1]));
}
DI float rdl(float a, int l) { return __uint_as_float((unsigned)__builtin_amdgcn_readlane((int)__float_as_uint(a), l)); }
DI void bc8(float A, float B, float (&o)[8]) {
  o[0] = rdl(A, 0); o[2] = rdl(A, 16); o[1] = rdl(A, 32); o[3] = rdl(A, 48);
  o[4] = rdl(B, 0); o[6] = rdl(B, 16); o[5] = rdl(B, 32); o[7] = rdl(B, 48);
}
DI void wait_cnt(GAS unsigned* c, unsigned have, unsigned target) {
  unsigned spins = 0;
#pragma unroll 1
  while (have < target) {
    __builtin_amdgcn_s_sleep(4);
    have = rfl(__hip_atomic_load(c, RLX_AGENT));
    if (++spins > SPIN_CAP) break;
  }
  __builtin_amdgcn_fence(__ATOMIC_ACQUIRE, "wavefront");
}
DI unsigned arrive(GAS unsigned* c, int lane) {
  unsigned r = 0;
  if (lane == 0) r = __hip_atomic_fetch_add(c, 1u, RLX_AGENT) + 1u;
  r = rfl(r);
  asm volatile("" ::"s"(r));
  return r;
}

__host__ __device__ constexpr int Dm(int m) { return m < 32 ? ((43 * (m & 15)) >> 4) + (m >> 4) : (4 + 8 * ((m - 32) >> 1) + 3 * ((m - 32) & 1) < 42 ? 4 + 8 * ((m - 32) >> 1) + 3 * ((m - 32) & 1) : 42); }

template <bool FULL, class MID, class HOOK, class ST>
DI u32x4 attention(const unsigned ka, const unsigned va, const bf16x8 q, const int n0, const int e, const int r, const int h, const bool helper, MID&& mid, HOOK&& hook, ST&& stamp) {
  bf16x8 kf[9];
#pragma unroll
  for (int n = 0; n < 9; ++n) kf[n] = ldsfrag(ka + n * 4096);
  f32x16 zero;
#pragma unroll
  for (int i = 0; i < 16; ++i) zero[i] = 0.f;
  f32x16 sm = MFMA(kf[8], q, zero);
  f32x16 sa, sb;
  if (FULL) {
    sb = MFMA(kf[0], q, zero);
    sa = MFMA(kf[1], q, zero);
    OPQ(sa);
  } else {
#pragma unroll
    for (int i = 0; i < 16; ++i) sb[i] = -1e30f;
  }
#pragma unroll
  for (int i = 0; i < 16; ++i) { const int kl = (i & 3) + 8 * (i >> 2) + 4 * h; sm[i] = (kl <= r) ? sm[i] : sb[i]; }
  float mx = sm[0], mx2 = sm[1];
#pragma unroll
  for (int i = 2; i < 16; i += 2) mx = fmaxf(fmaxf(mx, sm[i]), sm[i + 1]);
  if (FULL) {
    SCHED();
#pragma unroll
    for (int n = 1; n < 8; ++n) {
      if (n & 1) {
        if (n < 7) { sb = MFMA(kf[n + 1], q, zero); OPQ(sb); }
        SCHED();
#pragma unroll
        for (int i = 0; i < 16; i += 4) { mx = fmaxf(fmaxf(mx, sa[i]), sa[i + 1]); mx2 = fmaxf(fmaxf(mx2, sa[i + 2]), sa[i + 3]); }
      } else {
        sa = MFMA(kf[n + 1], q, zero); OPQ(sa);
        SCHED();
#pragma unroll
        for (int i = 0; i < 16; i += 4) { mx = fmaxf(fmaxf(mx, sb[i]), sb[i + 1]); mx2 = fmaxf(fmaxf(mx2, sb[i + 2]), sb[i + 3]); }
      }
      SCHED();
      if (n == 3) mid();
      if (n >= 4) hook(4 + n);
    }
  } else {
    mid();
#pragma unroll
    for (int n = 8; n < 12; ++n) hook(n);
#pragma unroll
    for (int n = 1; n < 8; ++n) {
      if (n >= n0) {
        const f32x16 s = MFMA(kf[n], q, zero);
#pragma unroll
        for (int i = 0; i < 16; ++i) mx = fmaxf(mx, s[i]);
      }
    }
  }
  mx = swap32_max(fmaxf(mx, mx2));
  f32x16 negm;
#pragma unroll
  for (int i = 0; i < 16; ++i) negm[i] = -mx;
  if (FULL) { sa = MFMA(kf[1], q, negm); OPQ(sa); }
  u32x4 p0, p1, d0, d1;
  {
    f32x16 p;
#pragma unroll
    for (int i = 0; i < 16; ++i) p[i] = __builtin_amdgcn_exp2f(sm[i] - mx);
    p0 = pack8(p, 0); p1 = pack8(p, 8);
#pragma unroll
    for (int k = 0; k < 4; ++k) {
      const int i0 = 2 * k, i1 = 8 + 2 * k;
      const int ka0 = (i0 & 3) + 8 * (i0 >> 2) + 4 * h, ka1 = (i1 & 3) + 8 * (i1 >> 2) + 4 * h;
      const unsigned m0 = (ka0 <= r ? 0xFFFFu : 0u) | (ka0 + 1 <= r ? 0xFFFF0000u : 0u);
      const unsigned m1 = (ka1 <= r ? 0xFFFFu : 0u) | (ka1 + 1 <= r ? 0xFFFF0000u : 0u);
      d0[k] = p0[k] & m0; d1[k] = p1[k] & m1;
    }
    p0 ^= d0; p1 ^= d1;
  }
  OPQ(p0); OPQ(p1); OPQ(d0); OPQ(d1);
  stamp(16);
  if (helper) VMWAIT(8);
  RBAR();
  stamp(7);
  const bool ones = ((r >> 4) != e);
  unsigned vp = ones ? L_ONES : va;
  const unsigned i1k = ones ? 0u : 1024u, i3k = ones ? 0u : 3072u;
  f32x16 o = zero;
  if (FULL) {
    const unsigned v8 = vp + 32 * i1k;
    const bf16x8 w80 = ldsfrag(v8), w81 = ldsfrag(v8 + i1k);
    const bf16x8 w00 = ldsfrag(vp); vp += i1k;
    const bf16x8 w01 = ldsfrag(vp); vp += i3k;
    bf16x8 v0 = ldsfrag(vp); vp += i1k;
    bf16x8 v1 = ldsfrag(vp); vp += i3k;
    o = MFMA(w80, asfrag(d0), o);
    o = MFMA(w81, asfrag(d1), o);
    o = MFMA(w00, asfrag(p0), o);
    o = MFMA(w01, asfrag(p1), o);
    OPQ(o);
    SCHED();
#pragma unroll
    for (int n = 1; n < 8; ++n) {
      bf16x8 n0f, n1f;
      if (n < 7) {
        if (n & 1) { sb = MFMA(kf[n + 1], q, negm); OPQ(sb); } else { sa = MFMA(kf[n + 1], q, negm); OPQ(sa); }
        n0f = ldsfrag(vp); vp += i1k;
        n1f = ldsfrag(vp); vp += i3k;
        hook(n - 1);
      }
      SCHED();
      f32x16& s = (n & 1) ? sa : sb;
#pragma unroll
      for (int i = 0; i < 8; ++i) s[i] = __builtin_amdgcn_exp2f(s[i]);
      MFMAP(o, v0, asfrag(pack8(s, 0)));
      SCHED();
#pragma unroll
      for (int i = 8; i < 16; ++i) s[i] = __builtin_amdgcn_exp2f(s[i]);
      MFMAP(o, v1, asfrag(pack8(s, 8)));
      SCHED();
      v0 = n0f; v1 = n1f;
    }
  } else {
#pragma unroll
    for (int n = 0; n < 6; ++n) hook(n);
    o = MFMA(ldsfrag(vp + 32 * i1k), asfrag(d0), o);
    o = MFMA(ldsfrag(vp + 33 * i1k), asfrag(d1), o);
#pragma unroll
    for (int n = 1; n < 8; ++n) {
      if (n >= n0) {
        f32x16 p = MFMA(kf[n], q, negm);
#pragma unroll
        for (int i = 0; i < 16; ++i) p[i] = __builtin_amdgcn_exp2f(p[i]);
        o = MFMA(ldsfrag(vp + (4 * n) * i1k), asfrag(pack8(p, 0)), o);
        o = MFMA(ldsfrag(vp + (4 * n + 1) * i1k), asfrag(pack8(p, 8)), o);
      }
    }
  }
  u32x4 of;
  if (e == 0) {
    const float il = __builtin_amdgcn_rcpf(o[8]);
#pragma unroll
    for (int i = 0; i < 8; ++i) o[i] *= il;
    of = pack8(o, 0);
    OPQ(of);
  } else {
    const float il = __builtin_amdgcn_rcpf(o[0]);
#pragma unroll
    for (int i = 8; i < 16; ++i) o[i] *= il;
    of = pack8(o, 8);
    OPQ(of);
  }
  return of;
}

DI void ln8(const u32x4* xr, const unsigned wb, const int lane) {
  float s1[8], s2[8], rs[8], nm[8];
  const bf16x2 one2 = __builtin_bit_cast(bf16x2, 0x3F803F80u);
#pragma unroll
  for (int i = 0; i < 8; ++i) {
    float a = 0.f, q = 0.f;
#pragma unroll
    for (int k = 0; k < 4; ++k) {
      const unsigned xu = xr[i][k];
      const bf16x2 v = __builtin_bit_cast(bf16x2, xu);
      a = __builtin_amdgcn_fdot2_f32_bf16(v, one2, a, false);
      q = __builtin_amdgcn_fdot2_f32_bf16(v, v, q, false);
    }
    s1[i] = a; s2[i] = q;
  }
  float A1, B1, A2, B2;
  red8(s1, A1, B1);
  red8(s2, A2, B2);
  A1 *= (1.f / 512.f); B1 *= (1.f / 512.f);
  A2 = __builtin_amdgcn_rsqf(fmaxf(fmaf(-A1, A1, A2 * (1.f / 512.f)), 0.f) + 1e-5f);
  B2 = __builtin_amdgcn_rsqf(fmaxf(fmaf(-B1, B1, B2 * (1.f / 512.f)), 0.f) + 1e-5f);
  bc8(A2, B2, rs);
  bc8(-A1 * A2, -B1 * B2, nm);
#pragma unroll
  for (int i = 0; i < 8; ++i) {
    u32x4 o;
#pragma unroll
    for (int k = 0; k < 4; ++k) o[k] = pk2(fmaf(blo(xr[i][k]), rs[i], nm[i]), fmaf(bhi(xr[i][k]), rs[i], nm[i]));
    ldsst(wb + (unsigned)((i ^ (lane & 7)) << 4), o);
  }
}
DI void poll_tag(const GAS u64* p, const u64 T) {
  unsigned spins = 0;
#pragma unroll 1
  for (;;) {
    const u64 v = __hip_atomic_load(p, RLX_AGENT);
    if (__all(v == T)) break;
    __builtin_amdgcn_s_sleep(1);
    if (++spins > SPIN_CAP) break;
  }
  asm volatile("" ::: "memory");
}
DI void signal(GAS unsigned* c, int lane) { if (lane == 0) (void)__hip_atomic_fetch_add(c, 1u, RLX_AGENT); }

__global__ void __launch_bounds__(512, 2)
fused_block(const void* __restrict__ gx, const void* __restrict__ gwqkv, const void* __restrict__ gwo, const void* __restrict__ gwg,
            const void* __restrict__ gwu, const void* __restrict__ gwd, void* __restrict__ gout, char* ws) {
  extern __shared__ __attribute__((aligned(16))) char smem[];
  const int lane = threadIdx.x & 63;
  const int wave = (int)rfl(threadIdx.x >> 6);
  STAMP(0);
  const int b = blockIdx.x, xcd = b & 7, idx = b >> 3;
  const int G = xcd * 2 + (idx >> 4), j = idx & 15, t = j >> 3, c = j & 7, H = 2 * G + t;
  const int r = lane & 31, h = lane >> 5;
  constexpr unsigned LB = 0;
  asm volatile("" ::"v"(smem));
  const rsrc_t rX = __builtin_amdgcn_make_buffer_rsrc((void*)gx, 0, 2048 * 512 * 2, 0x00020000);
  u32x4 xr[8];
  {
    const unsigned vo = lane * 16, so = (unsigned)(64 * H + 8 * wave) * 1024u;
#pragma unroll
    for (int i = 0; i < 8; ++i)
      asm volatile("buffer_load_dwordx4 %0, %1, %2, %3 offen offset:%4" : "=v"(xr[i]) : "v"(vo + (i >> 2) * 4096), "s"(rX), "s"(so), "i"((i & 3) * 1024) : "memory");
  }
  const rsrc_t rWQKV = __builtin_amdgcn_make_buffer_rsrc((void*)gwqkv, 0, 512 * 1536 * 2, 0x00020000);
  const rsrc_t rWO = __builtin_amdgcn_make_buffer_rsrc((void*)gwo, 0, 512 * 512 * 2, 0x00020000);
  const rsrc_t rWG = __builtin_amdgcn_make_buffer_rsrc((void*)gwg, 0, 512 * 1376 * 2, 0x00020000);
  const rsrc_t rWU = __builtin_amdgcn_make_buffer_rsrc((void*)gwu, 0, 512 * 1376 * 2, 0x00020000);
  const rsrc_t rWD = __builtin_amdgcn_make_buffer_rsrc((void*)gwd, 0, 1376 * 512 * 2, 0x00020000);
  const rsrc_t rOUT = __builtin_amdgcn_make_buffer_rsrc(gout, 0, 2048 * 512 * 2, 0x00020000);
  const rsrc_t rWS = __builtin_amdgcn_make_buffer_rsrc((void*)ws, 0, WS_BYTES, 0x00020000);

  const unsigned chsw = (unsigned)(((lane & 7) ^ (((lane >> 4) & 1) << 2)) * 16);
  const unsigned v1k = (unsigned)(lane >> 3) * 1024u + chsw;
  const int trq = (lane & 15) >> 2, trp = lane & 3, trcb = (lane >> 4) & 1;
  const int tau = (wave & 3) >> 1, hp = wave & 1;
  const int tt = 2 * H + tau;
  GAS unsigned* cntT = (GAS unsigned*)(ws + OFF_CNT + (unsigned)tt * 1024u);
  u32x4 qf;
  u32x2 xres[4];
  u64 T;

  const unsigned vq = (unsigned)(lane >> 3) * 3072u + chsw;
  {
    asm volatile("s_load_dwordx2 %0, %1, 0x0" : "=s"(T) : "s"(ws));
    if (j == 0 && wave == 4 && lane < 32) __hip_atomic_store((GAS unsigned*)(ws + OFF_CNT + (unsigned)(32 * G + lane) * 128u), 0u, RLX_AGENT);
    unsigned pf0;
    {
      u64 pc;
      asm volatile("s_getpc_b64 %0" : "=s"(pc));
      const u64 base = wave == 6 ? (pc & ~127ull) : (u64)gwqkv;
      const int n = wave == 0 ? 48 : wave == 6 ? 9 : 1;
      const int ln = lane < n ? lane : 0;
      const unsigned po = (unsigned)((wave == 6 ? idx : b) * n + ln) * 128u;
      asm volatile("global_load_dword %0, %1, %2" : "=v"(pf0) : "v"(po), "s"(base) : "memory");
    }
    RBAR();
    dma_run4(rWQKV, vq, (unsigned)(64 * wave) * 3072u + (512u + 64u * c) * 2u, 8 * 3072, LB + L_WK + (unsigned)(64 * wave) * 128u, 2);
    asm volatile("s_waitcnt vmcnt(8) lgkmcnt(0)" : "+v"(xr[0]), "+v"(xr[1]), "+v"(xr[2]), "+v"(xr[3]), "+v"(xr[4]), "+v"(xr[5]), "+v"(xr[6]), "+v"(xr[7]), "+s"(T), "+v"(pf0)::"memory");
    T += 1ull;
    STAMP(1);
    ln8(xr, LB + L_XLN + (unsigned)(wave >> 2) * 32768u + (unsigned)(lane >> 1) * 1024u + (unsigned)(lane & 1) * 512u + (unsigned)(wave & 3) * 128u, lane);
    STAMP(30);
  }
  const int Hp = H - 8 + wave;

  if (wave < 4) {
    unsigned pfd;
    {
      const int n = wave == 3 ? 16 : 43;
      const unsigned po = (unsigned)(b * n + (lane < n ? lane : 0)) * 128u;
      const rsrc_t rp = wave == 0 ? rWG : wave == 1 ? rWU : wave == 2 ? rWD : rWO;
      asm volatile("buffer_load_dword %0, %1, %2, 0 offen" : "=v"(pfd) : "v"(po), "s"(rp) : "memory");
    }
    dma_run4(rWQKV, vq, (unsigned)(64 * wave) * 3072u + (1024u + 64u * c) * 2u, 8 * 3072, LB + L_WV + (unsigned)(64 * wave) * 128u, 2);
    VMWAIT(9);
    BAR();
    STAMP(2);
    const unsigned trn = LB + (unsigned)((8 * h + trq) * 128 + ((hp ^ (trq >> 1)) * 64) + trcb * 32 + trp * 8);
    unsigned trn2 = trn + 65536u, trn3 = trn + L_WQA;
    OPQ(trn2); OPQ(trn3);
    bf16x8 xf[32];
    const unsigned xb = LB + L_XLN + (unsigned)tau * 32768u + (unsigned)h * 512u;
    auto ldx = [&](int s) { xf[s] = ldsfrag(xb + (unsigned)s * 1024u + (unsigned)((r ^ ((2 * (s & 3) + h) & 7)) << 4)); };
    f32x16 aK, aV, aQ;
#pragma unroll
    for (int i = 0; i < 16; ++i) { aK[i] = 0.f; aV[i] = 0.f; aQ[i] = 0.f; }
    u32x4 kf0, kf1, vf0, vf1;
#pragma unroll
    for (int s = 0; s < 4; ++s) ldx(s);
    const unsigned kvo = (unsigned)((tt * 8 + c) * 4 + hp * 2) * 1024u + lane * 16;
    pipe<32, 4>([&](auto I_) __attribute__((always_inline)) { constexpr int i = I_.value; return trfrag(trn + i * 2048, trn + i * 2048 + 512); },
                [&](auto I_, const bf16x8& w) __attribute__((always_inline)) {
                  constexpr int i = I_.value;
                  MFMAP(aK, w, xf[i]);
                  if constexpr (i + 4 < 32) ldx(i + 4);
                },
                [&](auto) {});
    kf0 = pack8(aK, 0); kf1 = pack8(aK, 8);
    __builtin_amdgcn_raw_buffer_store_b128(kf0, rWS, OFF_K + kvo, 0, 16);
    __builtin_amdgcn_raw_buffer_store_b128(kf1, rWS, OFF_K + kvo + 1024, 0, 16);
    STAMP(3);
    asm volatile("s_waitcnt vmcnt(0)" : "+v"(pfd)::"memory");
    if (lane == 0) __hip_atomic_store((GAS u64*)(ws + OFF_KF + (unsigned)(H * 8 + c) * 128u) + wave, T, RLX_AGENT);
    RBAR();
    STAMP(27);
    pipe<32, 5>([&](auto I_) __attribute__((always_inline)) { constexpr int i = I_.value; return trfrag(trn2 + i * 2048, trn2 + i * 2048 + 512); },
                [&](auto I_, const bf16x8& w) __attribute__((always_inline)) { MFMAP(aV, xf[I_.value], w); },
                [&](auto I_) __attribute__((always_inline)) { if (I_.value == 16) RBAR();   });
    vf0 = pack8(aV, 0); vf1 = pack8(aV, 8);
    __builtin_amdgcn_raw_buffer_store_b128(vf0, rWS, OFF_V + kvo, 0, 16);
    __builtin_amdgcn_raw_buffer_store_b128(vf1, rWS, OFF_V + kvo + 1024, 0, 16);
    STAMP(4);
    RBAR();
    pipe<32, 5>(
        [&](auto I_) __attribute__((always_inline)) {
          constexpr int i = I_.value;
          const unsigned a = i < 16 ? trn3 + i * 2048 : trn + L_WQB + (i - 16) * 2048;
          return trfrag(a, a + 512);
        },
        [&](auto I_, const bf16x8& w) __attribute__((always_inline)) {
          constexpr int i = I_.value;
          MFMAP(aQ, w, xf[i]);
          if (i == 18) {
            VMWAIT(0);
            if (lane == 0) __hip_atomic_store((GAS u64*)(ws + OFF_VF + (unsigned)(H * 8 + c) * 128u) + wave, T, RLX_AGENT);
          }
        },
        [&](auto) {});
    STAMP(5);
    RBAR();
#pragma unroll
    for (int i = 0; i < 16; ++i) aQ[i] *= 0.25f * 1.44269504088896f;
    qf = pack8(aQ, 0);
    const u32x4 qf1 = pack8(aQ, 8);
    ldsst(LB + L_K + (unsigned)((8 + tau) * 4 + hp * 2) * 1024u + lane * 16, kf0);
    ldsst(LB + L_K + (unsigned)((8 + tau) * 4 + hp * 2 + 1) * 1024u + lane * 16, kf1);
    ldsst(LB + L_V + (unsigned)((8 + tau) * 4 + hp * 2) * 1024u + lane * 16, vf0);
    ldsst(LB + L_V + (unsigned)((8 + tau) * 4 + hp * 2 + 1) * 1024u + lane * 16, vf1);
    ldsst(LB + L_QH + (unsigned)wave * 1024u + lane * 16, qf1);
    { const u32x4 one4 = {0x3F803F80u, 0x3F803F80u, 0x3F803F80u, 0x3F803F80u}; ldsst(LB + L_ONES, one4); }
#pragma unroll
    for (int g = 0; g < 4; ++g)
      xres[g] = __builtin_amdgcn_raw_buffer_load_b64(rX, (unsigned)((32 * tt + r) * 1024 + (64 * c + 32 * hp + 8 * g + 4 * h) * 2), 0, 0);
    BAR();
  } else {
    const int hw = wave - 4;
    VMWAIT(0);
    BAR();
    if (j == 0 && hw == 0 && lane == 0) __hip_atomic_store((GAS u64*)(ws + OFF_READY + (unsigned)G * 256u), T, RLX_AGENT);
    u32x4 st[16];
#pragma unroll
    for (int i = 0; i < 16; ++i) {
      const unsigned so = (unsigned)((i < 8 ? 256 : 0) + 64 * hw + 8 * (i & 7)) * 3072u + ((i < 8 ? 1024u : 0u) + 64u * c) * 2u;
      asm volatile("buffer_load_dwordx4 %0, %1, %2, %3 offen" : "=v"(st[i]) : "v"(vq), "s"(rWQKV), "s"(so) : "memory");
    }
    STAMP(3);
    RBAR();
    dma_run4(rWQKV, vq, (unsigned)(256 + 64 * hw) * 3072u + (64u * c) * 2u, 8 * 3072, LB + L_WQB + (unsigned)(64 * hw) * 128u, 2);
    asm volatile("s_waitcnt vmcnt(16)" : "+v"(st[0]), "+v"(st[1]), "+v"(st[2]), "+v"(st[3]), "+v"(st[4]), "+v"(st[5]), "+v"(st[6]), "+v"(st[7])::"memory");
#pragma unroll
    for (int i = 0; i < 8; ++i) ldsst(LB + L_WV + (unsigned)(256 + 64 * hw + 8 * i) * 128u + lane * 16, st[i]);
    BAR();
    asm volatile("s_waitcnt vmcnt(8)" : "+v"(st[8]), "+v"(st[9]), "+v"(st[10]), "+v"(st[11]), "+v"(st[12]), "+v"(st[13]), "+v"(st[14]), "+v"(st[15])::"memory");
#pragma unroll
    for (int i = 0; i < 8; ++i) ldsst(LB + L_WQA + (unsigned)(64 * hw + 8 * i) * 128u + lane * 16, st[8 + i]);
    VMWAIT(0);
    BAR();
    if (Hp >= 0) {
      poll_tag((const GAS u64*)(ws + OFF_KF + (unsigned)(Hp * 8 + c) * 128u) + (lane & 3), T);
      dma_run4_sc1(rWS, lane * 16, OFF_K + (unsigned)((2 * Hp) * 8 + c) * 4096u, LB + L_K + (unsigned)(8 * hw) * 1024u);
      dma_run4_sc1(rWS, lane * 16, OFF_K + (unsigned)((2 * Hp + 1) * 8 + c) * 4096u, LB + L_K + (unsigned)(8 * hw + 4) * 1024u);
    }
    STAMP(5);
    RBAR();
    VMWAIT(0);
    RBAR();
    qf = *(LAS u32x4*)(LB + L_QH + (unsigned)hw * 1024u + lane * 16);
#pragma unroll
    for (int g = 0; g < 4; ++g) { xres[g][0] = 0u; xres[g][1] = 0u; }
  }
  STAMP(6);

  const int du0 = (43 * j) >> 4, ndu = ((43 * (j + 1)) >> 4) - du0;
  unsigned v3g[3], v3u[3];
#pragma unroll
  for (int i = 0; i < 3; ++i) {
    const unsigned qd = (unsigned)(i * 64 + lane), row = qd / 12u, ch = qd - row * 12u;
    v3g[i] = row * 2752u + ch * 16u;
    v3u[i] = row * 2752u + (ch ^ 2u) * 16u;
  }
  {
    const int e = wave >> 2;
    const unsigned ka = LB + L_K + (unsigned)(tau * 4 + hp * 2 + e) * 1024u + lane * 16;
    const unsigned va = LB + L_V + (unsigned)(tau * 4 + hp * 2) * 1024u + lane * 16;
    const unsigned so0 = (unsigned)(16 * wave) * 2752u + 64u * du0, ld0 = LB + (unsigned)(16 * wave) * 192u;
    auto hook = [&](int n) {
      if (n < 3) { dma16(rWG, v3g[n], so0, ld0 + n * 1024u); dma16(rWU, v3u[n], so0, ld0 + 24576u + n * 1024u); }
      else if (n >= 8) {
        const unsigned row0 = (unsigned)(64 * wave + 16 * (n - 8));
        dma16(rWO, v1k, row0 * 1024u + 128u * c, LB + L_WO + row0 * 128u);
        dma16(rWO, v1k, (row0 + 8u) * 1024u + 128u * c, LB + L_WO + (row0 + 8u) * 128u);
      }
    };
    auto mid = [&]() {
      if (wave >= 4) {
        const int hw = wave - 4;
        poll_tag((lane == 4 || Hp < 0) ? (const GAS u64*)(ws + OFF_READY + (unsigned)G * 256u) : (const GAS u64*)(ws + OFF_VF + (unsigned)(Hp * 8 + c) * 128u) + (lane & 3), T);
        if (Hp >= 0) {
          dma_run4_sc1(rWS, lane * 16, OFF_V + (unsigned)((2 * Hp) * 8 + c) * 4096u, LB + L_V + (unsigned)(8 * hw) * 1024u);
          dma_run4_sc1(rWS, lane * 16, OFF_V + (unsigned)((2 * Hp + 1) * 8 + c) * 4096u, LB + L_V + (unsigned)(8 * hw + 4) * 1024u);
        }
      }
    };
    auto st = [&](int i) { STAMP(i); };
    u32x4 of;
    if (tt >= 8) of = attention<true>(ka, va, asfrag(qf), 0, e, r, h, wave >= 4, mid, hook, st);
    else of = attention<false>(ka, va, asfrag(qf), 8 - tt, e, r, h, wave >= 4, mid, hook, st);
    __builtin_amdgcn_raw_buffer_store_b128(of, rWS, OFF_O + (unsigned)(tt * 32 + 4 * c + 2 * hp + e) * 1024u + lane * 16, 0, 16);
  }
  STAMP(17);
  const unsigned trpm = (unsigned)((4 * h + trq) * 128 + trcb * 32 + trp * 8);

  if (wave < 4) {
    const int kh = hp;
    VMWAIT(0);
    const unsigned haveO = arrive(cntT, lane);
    RBAR();
    STAMP(8);
    wait_cnt(cntT, haveO, 32u);
    STAMP(9);
    u32x4 of[16];
#pragma unroll
    for (int s = 0; s < 16; ++s) of[s] = __builtin_amdgcn_raw_buffer_load_b128(rWS, OFF_O + (unsigned)(tt * 32 + 16 * kh + s) * 1024u + lane * 16, 0, 16);
    const unsigned aA = LB + L_WO + (unsigned)kh * 32768u + trpm + (unsigned)((kh ^ (trq >> 1)) * 64);
    const unsigned aB = aA ^ 64u;
    f32x16 accA, accB;
#pragma unroll
    for (int i = 0; i < 16; ++i) { accA[i] = 0.f; accB[i] = 0.f; }
    pipe<32, 6>([&](auto I_) { constexpr int i = I_.value; const unsigned a = ((i & 1) ? aA : aB) + (i >> 1) * 2048; return trfrag(a, a + 1024); },
                [&](auto I_, const bf16x8& w) { constexpr int i = I_.value; if (i & 1) MFMAP(accA, w, asfrag(of[i >> 1])); else MFMAP(accB, w, asfrag(of[i >> 1])); },
                [&](auto) {});
    STAMP(18);
    RBAR();
#pragma unroll
    for (int g = 0; g < 4; ++g) { f32x4 v = {accB[4 * g], accB[4 * g + 1], accB[4 * g + 2], accB[4 * g + 3]}; *(LAS f32x4*)(LB + L_SCR + (unsigned)wave * 4096u + g * 1024 + lane * 16) = v; }
    BAR();
    unsigned hres[8];
    {
      float hv[16];
#pragma unroll
      for (int g = 0; g < 4; ++g) {
        const f32x4 v = *(LAS f32x4*)(LB + L_SCR + (unsigned)(wave ^ 1) * 4096u + g * 1024 + lane * 16);
        hres[2 * g] = pk2(accA[4 * g] + v[0] + blo(xres[g][0]), accA[4 * g + 1] + v[1] + bhi(xres[g][0]));
        hres[2 * g + 1] = pk2(accA[4 * g + 2] + v[2] + blo(xres[g][1]), accA[4 * g + 3] + v[3] + bhi(xres[g][1]));
      }
      const unsigned o = OFF_H + (unsigned)(tt * 32 + 4 * c + 2 * kh) * 1024u + lane * 16;
      u32x4 h0 = {hres[0], hres[1], hres[2], hres[3]}, h1 = {hres[4], hres[5], hres[6], hres[7]};
      __builtin_amdgcn_raw_buffer_store_b128(h0, rWS, o, 0, 16);
      __builtin_amdgcn_raw_buffer_store_b128(h1, rWS, o + 1024, 0, 16);
      float sum = 0.f;
#pragma unroll
      for (int i = 0; i < 8; ++i) { hv[2 * i] = blo(hres[i]); hv[2 * i + 1] = bhi(hres[i]); sum += hv[2 * i] + hv[2 * i + 1]; }
      const float mean = swap32_add(sum) * (1.f / 32.f);
      float m2 = 0.f;
#pragma unroll
      for (int i = 0; i < 16; ++i) { const float d = hv[i] - mean; m2 = fmaf(d, d, m2); }
      m2 = swap32_add(m2);
      u32x2 st = {__float_as_uint(mean), __float_as_uint(m2)};
      if (h == 0) __builtin_amdgcn_raw_buffer_store_b64(st, rWS, OFF_HS + (unsigned)((tt * 16 + 2 * c + kh) * 256 + r * 8), 0, 16);
    }
    STAMP(19);
    VMWAIT(0);
    RBAR();
    (void)arrive(cntT + 32, lane);
    STAMP(10);

    const int t4 = 4 * G + wave;
    GAS unsigned* cnt4 = (GAS unsigned*)(ws + OFF_CNT + (unsigned)t4 * 1024u);
    wait_cnt(cnt4 + 32, 0u, 16u);
    STAMP(11);
    const unsigned hb = OFF_H + (unsigned)t4 * 32768u + lane * 16;
    u32x4 hf[32];
#pragma unroll
    for (int s = 0; s < 12; ++s) hf[s] = __builtin_amdgcn_raw_buffer_load_b128(rWS, hb + s * 1024, 0, 16);
    u32x2 stp[8];
    const unsigned w3e = LB + (unsigned)(trcb * 24576 + (4 * h + trq) * 192 + trp * 8 + trcb * 32);
    const unsigned w3o = LB + (unsigned)(trcb * 24576 + (4 * h + trq) * 192 + trp * 8 - trcb * 32);
    unsigned w3e1 = w3e + 49152u, w3o1 = w3o + 49152u, w3e2 = w3e + 98304u, w3o2 = w3o + 98304u, csa = LB + L_SCR + (unsigned)r * 16u;
    OPQ(w3e1); OPQ(w3o1); OPQ(w3e2); OPQ(w3o2); OPQ(csa);
    f32x16 ac[6];
#pragma unroll
    for (int u = 0; u < 6; ++u)
#pragma unroll
      for (int i = 0; i < 16; ++i) ac[u][i] = 0.f;
    float rsg, irs;
    u32x4 mfr;
    float tg[8];
    const unsigned hho = OFF_HH + (unsigned)(t4 * 86 + 2 * du0) * 1024u + lane * 16;
    const unsigned hho2 = ndu == 3 ? hho + 4096u : 0x7F000000u;
    auto epi = [&](int u, int k) {
      if (k == 1) {
#pragma unroll
        for (int i = 0; i < 8; ++i) tg[i] = ac[u][i] * rsg;
      } else if (k == 2 || k == 3) {
#pragma unroll
        for (int i = 4 * (k - 2); i < 4 * (k - 1); ++i) tg[i] = __builtin_amdgcn_exp2f(tg[i]);
      } else if (k == 4) {
#pragma unroll
        for (int i = 0; i < 8; ++i) tg[i] = fmaf(tg[i], irs, irs);
      } else if (k == 5 || k == 6) {
#pragma unroll
        for (int i = 4 * (k - 5); i < 4 * (k - 4); ++i) tg[i] = __builtin_amdgcn_rcpf(tg[i]);
      } else if (k == 7) {
#pragma unroll
        for (int i = 0; i < 8; ++i) tg[i] *= ac[u][8 + i];
      } else if (k == 8) {
        u32x4 o = {pk2(tg[0], tg[1]), pk2(tg[2], tg[3]), pk2(tg[4], tg[5]), pk2(tg[6], tg[7])};
        __builtin_amdgcn_raw_buffer_store_b128(o, rWS, u < 4 ? hho + u * 1024 : hho2 + (u - 4) * 1024, 0, 16);
      }
      if (k >= 1 && k <= 7) {
#pragma unroll
        for (int i = 0; i < 8; ++i) OPQ(tg[i]);
      }
    };
    pipe<198, 6>(
        [&](auto I_) __attribute__((always_inline)) {
          constexpr int i = I_.value;
          if constexpr (i < 144) {
            constexpr int u = i % 6, s = (i % 48) / 6;
            const unsigned a = ((i / 48 == 1) ? ((u & 1) ? w3o1 : w3e1) : ((u & 1) ? w3o : w3e)) + s * 3072 + u * 32;
            return trfrag(a, a + 1536);
          } else {
            constexpr int u = (i - 144) / 9, s = (i - 144) % 9;
            if constexpr (s == 8) return ldsfrag(csa + u * 512);
            else { const unsigned a = ((u & 1) ? w3o2 : w3e2) + s * 3072 + u * 32; return trfrag(a, a + 1536); }
          }
        },
        [&](auto I_, const bf16x8& w) __attribute__((always_inline)) {
          constexpr int i = I_.value;
          if constexpr (i < 144) {
            constexpr int u = i % 6, ks = i / 6, s = ks < 16 ? ks : ks + 8;
            MFMAP(ac[u], w, asfrag(hf[s]));
            if constexpr (u == 5 && ks + 12 < 32) { constexpr int k2 = ks + 12, s2 = k2 < 16 ? k2 : k2 < 24 ? k2 + 8 : k2 - 8; hf[s2] = __builtin_amdgcn_raw_buffer_load_b128(rWS, hb + s2 * 1024, 0, 16); }
            if (i == 121) {
#pragma unroll
              for (int q = 0; q < 8; ++q) stp[q] = __builtin_amdgcn_raw_buffer_load_b64(rWS, OFF_HS + (unsigned)((t4 * 16 + 2 * q + h) * 256 + r * 8), 0, 16);
            }
          } else {
            constexpr int u = (i - 144) / 9, s = (i - 144) % 9;
            if constexpr (s == 8) MFMAP(ac[u], w, asfrag(mfr));
            else MFMAP(ac[u], w, asfrag(hf[16 + s]));
            if (i == 146) {
              float ms = 0.f, q = 0.f;
#pragma unroll
              for (int k = 0; k < 8; ++k) { ms += __uint_as_float(stp[k][0]); q += __uint_as_float(stp[k][1]); }
              const float mean = swap32_add(ms) * (1.f / 16.f);
#pragma unroll
              for (int k = 0; k < 8; ++k) { const float d = __uint_as_float(stp[k][0]) - mean; q = fmaf(32.f * d, d, q); }
              q = swap32_add(q) * (1.f / 512.f) + 1e-5f;
              const float rs = __builtin_amdgcn_rsqf(q);
              rsg = rs * -1.44269504088896f;
              irs = q * rs;
              const unsigned a1 = pk2(-mean, 0.f) & 0xFFFFu;
              const float r1 = -mean - blo(a1);
              const unsigned a2 = pk2(r1, 0.f) & 0xFFFFu;
              const unsigned a3 = pk2(r1 - blo(a2), 0.f) & 0xFFFFu;
              mfr[0] = h ? 0u : (a1 | (a2 << 16)); mfr[1] = h ? 0u : (a1 | (a3 << 16)); mfr[2] = h ? 0u : (a2 | (a1 << 16)); mfr[3] = 0u;
            }
            if constexpr (u > 0) epi(u - 1, s);
            if (i == 144 + 43) { VMWAIT(1); signal(cnt4 + 64, lane); }
          }
        },
        [&](auto I_) __attribute__((always_inline)) {
          constexpr int i = I_.value;
          if (i == 54) RBAR();
          if (i == 96) { STAMP(21); RBAR(); STAMP(22); }
          if (i == 150) { RBAR(); STAMP(23); }
        });
    STAMP(12);
#pragma unroll
    for (int k = 1; k < 9; ++k) epi(5, k);
    VMWAIT(2);
    signal(cnt4 + 96, lane);
    VMWAIT(1);
    if (ndu == 3) signal(cnt4 + 128, lane);
    RBAR();
    STAMP(13);

    const unsigned gb = OFF_HH + (unsigned)(tt * 86 + kh) * 1024u + lane * 16;
    u32x4 gf[43];
    wait_cnt(cntT + 64, 0u, 16u);
    STAMP(14);
#pragma unroll
    for (int m = 0; m < 16; ++m) gf[m] = __builtin_amdgcn_raw_buffer_load_b128(rWS, gb + Dm(m) * 2048, 0, 16);
    STAMP(24);
    RBAR();
    STAMP(25);
#pragma unroll
    for (int m = 16; m < 36; ++m) gf[m] = __builtin_amdgcn_raw_buffer_load_b128(rWS, gb + Dm(m) * 2048, 0, 16);
    const unsigned dA = LB + (unsigned)kh * 2048u + trpm + (unsigned)((kh ^ (trq >> 1)) * 64);
    const unsigned dB = dA ^ 64u;
    unsigned dA1 = dA + 49152u, dB1 = dB + 49152u, dA2 = dA + 98304u, dB2 = dB + 98304u;
    OPQ(dA1); OPQ(dB1); OPQ(dA2); OPQ(dB2);
#pragma unroll
    for (int i = 0; i < 16; ++i) { accA[i] = 0.f; accB[i] = 0.f; }
    pipe<86, 6>(
        [&](auto I_) __attribute__((always_inline)) {
          constexpr int i = I_.value;
          constexpr int m = i >> 1, p = m / 12, mm = m % 12;
          const unsigned a = ((i & 1) ? ((p == 0 || p == 3) ? dA1 : p == 1 ? dA : dA2) : ((p == 0 || p == 3) ? dB1 : p == 1 ? dB : dB2)) + mm * 4096;
          return trfrag(a, a + 1024);
        },
        [&](auto I_, const bf16x8& w) __attribute__((always_inline)) {
          constexpr int i = I_.value;
          if (i & 1) MFMAP(accA, w, asfrag(gf[i >> 1])); else MFMAP(accB, w, asfrag(gf[i >> 1]));
          if constexpr ((i & 1) && (i >> 1) < 7) gf[36 + (i >> 1)] = __builtin_amdgcn_raw_buffer_load_b128(rWS, gb + Dm(36 + (i >> 1)) * 2048, 0, 16);
        },
        [&](auto I_) __attribute__((always_inline)) {
          constexpr int i = I_.value;
          if (i == 30) RBAR();
          if (i == 48) RBAR();
          if (i == 72) RBAR();
        });
    STAMP(26);
    RBAR();
    {
      const unsigned ob = LB + (unsigned)kh * 17408u + (unsigned)(32 * tau + r) * 272u + (unsigned)h * 16u;
#pragma unroll
      for (int g = 0; g < 4; ++g) {
        const f32x4 va = {accA[4 * g] + blo(hres[2 * g]), accA[4 * g + 1] + bhi(hres[2 * g]), accA[4 * g + 2] + blo(hres[2 * g + 1]), accA[4 * g + 3] + bhi(hres[2 * g + 1])};
        const f32x4 vb = {accB[4 * g], accB[4 * g + 1], accB[4 * g + 2], accB[4 * g + 3]};
        *(LAS f32x4*)(ob + (unsigned)kh * 128u + g * 32) = va;
        *(LAS f32x4*)(ob + (unsigned)(kh ^ 1) * 128u + g * 32) = vb;
      }
    }
    BAR();
    STAMP(15);
  } else {
    const int hw = wave - 4;
    auto w3blk = [&](int blk, unsigned dst) {
#pragma unroll 1
      for (int p = 0; p < 2; ++p) {
        const unsigned row0 = (unsigned)(32 * hw + 16 * p), so = (128u * blk + row0) * 2752u + 64u * du0, ld = LB + dst + row0 * 192u;
#pragma unroll
        for (int i = 0; i < 3; ++i) {
          dma16(rWG, v3g[i], so, ld + i * 1024u);
          dma16(rWU, v3u[i], so, ld + 24576u + i * 1024u);
        }
      }
    };
    VMWAIT(0);
    signal(cntT, lane);
    RBAR();
    w3blk(1, 49152);
    u32x4 b3[12];
#pragma unroll
    for (int q = 0; q < 12; ++q) {
      const unsigned so = (384u + (unsigned)(32 * hw + 16 * (q / 6))) * 2752u + 64u * du0;
      if ((q % 6) < 3) asm volatile("buffer_load_dwordx4 %0, %1, %2, %3 offen" : "=v"(b3[q]) : "v"(v3g[q % 3]), "s"(rWG), "s"(so) : "memory");
      else asm volatile("buffer_load_dwordx4 %0, %1, %2, %3 offen" : "=v"(b3[q]) : "v"(v3u[q % 3]), "s"(rWU), "s"(so) : "memory");
    }
    VMWAIT(12);
    RBAR();
    RBAR();
    RBAR();
    w3blk(2, 98304);
    asm volatile("s_waitcnt vmcnt(0)" : "+v"(b3[0]), "+v"(b3[1]), "+v"(b3[2]), "+v"(b3[3]), "+v"(b3[4]), "+v"(b3[5]), "+v"(b3[6]), "+v"(b3[7]), "+v"(b3[8]), "+v"(b3[9]), "+v"(b3[10]), "+v"(b3[11])::"memory");
    if (hw == (G & 3)) {
      const int blk = G >> 2;
      if (blk == 3) {
#pragma unroll
        for (int q = 0; q < 12; ++q) ldsst(LB + L_SCR + (unsigned)(16 * (q / 6)) * 192u + ((q % 6) >= 3 ? 6144u : 0u) + (unsigned)(q % 3) * 1024u + lane * 16, b3[q]);
        asm volatile("s_waitcnt lgkmcnt(0)" ::: "memory");
      }
      const unsigned we = LB + (blk == 3 ? L_SCR + (unsigned)trcb * 6144u : (unsigned)(blk * 49152 + trcb * 24576 + 32 * hw * 192)) + (unsigned)((4 * h + trq) * 192 + trp * 8 + trcb * 32);
      const unsigned wo = we - (unsigned)(trcb * 64);
      f32x16 cs;
#pragma unroll
      for (int i = 0; i < 16; ++i) cs[i] = 0.f;
      bf16x8 f[12];
#pragma unroll
      for (int q = 0; q < 12; ++q) { const int u = q >> 1; const unsigned a = ((u & 1) ? wo : we) + (q & 1) * 3072 + u * 32; f[q] = trfrag(a, a + 1536); }
#pragma unroll
      for (int q = 0; q < 12; ++q) {
        const unsigned sel = (r == (q >> 1)) ? 0x3F803F80u : 0u;
        const u32x4 sv = {sel, sel, sel, sel};
        cs = MFMA(f[q], asfrag(sv), cs);
      }
      if (r < 6) {
        const unsigned tg32 = (unsigned)T;
#pragma unroll
        for (int g = 0; g < 16; ++g)
          __hip_atomic_store((GAS u64*)(ws + OFF_CS2 + (unsigned)((j * 16 + G) * 1536 + (r * 32 + (g >> 2) * 8 + h * 4 + (g & 3)) * 8)), ((u64)tg32 << 32) | __float_as_uint(cs[g]), RLX_AGENT);
      }
    }
    if (b == 0 && hw == 3) {
      unsigned spins = 0;
#pragma unroll 1
      for (;;) {
        bool ok = true;
#pragma unroll
        for (int i = 0; i < 4; ++i) ok &= __hip_atomic_load((const GAS u64*)(ws + OFF_KF + (unsigned)(i * 64 + lane) * 128u), RLX_AGENT) == T;
        if (__all(ok)) break;
        __builtin_amdgcn_s_sleep(2);
        if (++spins > SPIN_CAP) break;
      }
      if (lane == 0) __hip_atomic_store((GAS u64*)(ws + OFF_EPOCH), T, RLX_AGENT);
    }
    STAMP(20);
    RBAR();
#pragma unroll
    for (int q = 0; q < 12; ++q)
      ldsst(LB + (unsigned)(32 * hw + 16 * (q / 6)) * 192u + ((q % 6) >= 3 ? 24576u : 0u) + (unsigned)(q % 3) * 1024u + lane * 16, b3[q]);
    STAMP(21);
    BAR();
    if (hw < 3) {
      float pv[16];
      unsigned spins = 0;
#pragma unroll 1
      for (;;) {
        bool ok = true;
#pragma unroll
        for (int g = 0; g < 16; ++g) {
          const u64 x = __hip_atomic_load((const GAS u64*)(ws + OFF_CS2 + (unsigned)((j * 16 + g) * 1536 + (hw * 64 + lane) * 8)), RLX_AGENT);
          pv[g] = __uint_as_float((unsigned)x);
          ok &= (unsigned)(x >> 32) == (unsigned)T;
        }
        if (__all(ok)) break;
        __builtin_amdgcn_s_sleep(2);
        if (++spins > SPIN_CAP) break;
      }
      const float v = (((pv[0] + pv[1]) + (pv[2] + pv[3])) + ((pv[4] + pv[5]) + (pv[6] + pv[7]))) + (((pv[8] + pv[9]) + (pv[10] + pv[11])) + ((pv[12] + pv[13]) + (pv[14] + pv[15])));
      const unsigned c1 = pk2(v, 0.f) & 0xFFFFu;
      const float r1 = v - blo(c1);
      const unsigned c2 = pk2(r1, 0.f) & 0xFFFFu;
      const unsigned c3 = pk2(r1 - blo(c2), 0.f) & 0xFFFFu;
      const u32x4 f = {c1 | (c1 << 16), c2 | (c1 << 16), c2 | (c3 << 16), 0u};
      ldsst(LB + L_SCR + (unsigned)(hw * 64 + lane) * 16u, f);
    }
    BAR();
    auto w4 = [&](int m0, int n, unsigned dst) {
#pragma unroll 1
      for (int q = 0; q < n; ++q) {
        const int mm = hw + 4 * q;
        dma_run4(rWD, v1k, (unsigned)Dm(m0 + mm) * 32768u + 128u * c, 8192, LB + dst + (unsigned)mm * 4096u, 1);
      }
    };
    w4(0, 3, 49152);
    w4(12, 3, 0);
    RBAR();
    w4(24, 3, 98304);
    wait_cnt(cntT + 96 + 32 * hp, 0u, hp ? 11u : 16u);
    VMWAIT(0);
    RBAR();
    RBAR();
    w4(36, 2, 49152);
    VMWAIT(8);
    RBAR();
    VMWAIT(0);
    RBAR();
    RBAR();
    RBAR();
  }
  {
    const int row = threadIdx.x >> 3, ch = threadIdx.x & 7;
    const unsigned a = LB + (unsigned)row * 272u + (unsigned)ch * 32u;
    const f32x4 p0 = *(LAS f32x4*)a + *(LAS f32x4*)(a + 17408), p1 = *(LAS f32x4*)(a + 16) + *(LAS f32x4*)(a + 17408 + 16);
    const u32x4 o = {pk2(p0[0], p0[1]), pk2(p0[2], p0[3]), pk2(p1[0], p1[1]), pk2(p1[2], p1[3])};
    __builtin_amdgcn_raw_buffer_store_b128(o, rOUT, (unsigned)((64 * H + row) * 1024 + 128 * c + ch * 16), 0, 16);
  }
  STAMP_END();
}

extern "C" void kernel_launch(void* const* d_in, const int* in_sizes, int n_in, void* d_out, int out_size, void* d_ws, size_t ws_size,
                              hipStream_t stream) {
  (void)in_sizes; (void)n_in; (void)out_size; (void)ws_size;
  (void)hipFuncSetAttribute((const void*)fused_block, hipFuncAttributeMaxDynamicSharedMemorySize, (int)LDS_BYTES);
  hipLaunchKernelGGL(fused_block, dim3(256), dim3(512), LDS_BYTES, stream, d_in[0], d_in[1], d_in[2], d_in[4], d_in[5], d_in[6], d_out, (char*)d_ws);
}
```
